```python
import torch

def _softmax(x, axis=-1):
    return torch.softmax(x, dim=axis)
def _gelu(x):
    return torch.nn.functional.gelu(x, approximate="tanh")
def _silu(x):
    return torch.nn.functional.silu(x)
def _sigmoid(x):
    return torch.sigmoid(x)
def _layernorm(x, axis=-1, eps=1e-5):
    m = x.mean(dim=axis, keepdim=True)
    v = x.var(dim=axis, keepdim=True, unbiased=False)
    return (x - m) / torch.sqrt(v + eps)
def _scatter_add(tgt, idx, src, axis=0):
    return tgt.clone().index_add_(axis, idx, src)
def _segment_sum(src, seg, n):
    out = torch.zeros((n,) + src.shape[1:], dtype=src.dtype)
    return out.index_add_(0, seg, src)
def _pad(x, pw):
    flat = tuple(v for pair in reversed(pw) for v in pair)
    return torch.nn.functional.pad(x, flat)

kernel_name = 'sequential_preLN__mha_block__mixer_channel_mlp__prompt'

def setup_inputs():

    _w = (lambda a: (lambda t: t.to(torch.bfloat16) if t.is_floating_point() else t)(torch.as_tensor(a)))
    def _A_setup():
        B, S, D, H = 1, 2048, 512, 32
        return {
            "x": _w((lambda *s: torch.randn(*s, dtype=torch.bfloat16) * 0.05)(B, S, D)),
            "wqkv": _w((lambda *s: torch.randn(*s, dtype=torch.bfloat16) * 0.05)(D, 3 * D)),
            "wo": _w((lambda *s: torch.randn(*s, dtype=torch.bfloat16) * 0.05)(D, D)),
        }
    _A_in = _A_setup()

    def _B_setup():
        B, S, D, F = 1, 2048, 512, 1792
        return {
            "x": _w((lambda *s: torch.randn(*s, dtype=torch.bfloat16) * 0.05)(B, S, D)),
            "w1": _w((lambda *s: torch.randn(*s, dtype=torch.bfloat16) * 3.2)(D, F) * (D ** -0.5)),
            "b1": _w((lambda *s: torch.randn(*s, dtype=torch.bfloat16) * 1.6)(F)),
            "w2": _w((lambda *s: torch.randn(*s, dtype=torch.bfloat16) * 0.05)(F, D) * (F ** -0.5)),
            "b2": _w((lambda *s: torch.randn(*s, dtype=torch.bfloat16) * 0.1)(D)),
        }
    _B_in = _B_setup()

    return {
        "A_x": _A_in["x"],
        "A_wqkv": _A_in["wqkv"],
        "A_wo": _A_in["wo"],
        "B_x": _B_in["x"],
        "B_w1": _B_in["w1"],
        "B_b1": _B_in["b1"],
        "B_w2": _B_in["w2"],
        "B_b2": _B_in["b2"],
    }

def reference(A_x, A_wqkv, A_wo, B_x, B_w1, B_b1, B_w2, B_b2):
    def _A_ref(x, wqkv, wo):
        B, S, D = x.shape
        H = 32
        Dh = D // H
        qkv = (lambda x, *d: x.permute(*d))((x @ wqkv).reshape(B, S, 3, H, Dh), 2, 0, 3, 1, 4)
        q, k, v = qkv[0], qkv[1], qkv[2]
        scores = torch.einsum("bhqd,bhkd->bhqk", q, k) * (Dh ** -0.5)
        p = _softmax(scores, axis=-1)
        o = torch.einsum("bhqk,bhkd->bhqd", p, v)
        return (lambda x, *d: x.permute(*d))(o, 0, 2, 1, 3).reshape(B, S, D) @ wo

    def _B_ref(x, w1, b1, w2, b2):
        y = _layernorm(x, axis=-1)
        h = _gelu(torch.einsum("bsd,df->bsf", y, w1) + b1)
        return torch.einsum("bsf,fd->bsd", h, w2) + b2

    x = A_x
    h = x + _A_ref(_layernorm(x, axis=-1), A_wqkv, A_wo)
    h2 = h + _B_ref(_layernorm(h, axis=-1), B_w1, B_b1, B_w2, B_b2)
    return h2
```

```cpp
#include <hip/hip_runtime.h>
#include <hip/hip_bf16.h>

typedef unsigned v4u __attribute__((ext_vector_type(4)));
typedef unsigned v8u __attribute__((ext_vector_type(8)));
typedef unsigned v16u __attribute__((ext_vector_type(16)));
typedef unsigned v2u __attribute__((ext_vector_type(2)));
typedef float f32x16 __attribute__((ext_vector_type(16)));
typedef float f32x8 __attribute__((ext_vector_type(8)));
typedef float f32x4 __attribute__((ext_vector_type(4)));
typedef float f32x2 __attribute__((ext_vector_type(2)));
typedef __bf16 bf16x2 __attribute__((ext_vector_type(2)));
typedef __bf16 bf16x8 __attribute__((ext_vector_type(8)));
typedef __bf16 bf16x16 __attribute__((ext_vector_type(16)));
typedef short s16x4 __attribute__((ext_vector_type(4)));
typedef unsigned long long u64;
#define LAS __attribute__((address_space(3)))
#define GAS __attribute__((address_space(1)))
#if defined(__HIP_DEVICE_COMPILE__)
#define NOPK __attribute__((target("no-packed-fp32-ops")))
#else
#define NOPK
#endif
#define DEVI __device__ __forceinline__
typedef __amdgpu_buffer_rsrc_t rsrc_t;
DEVI float u2f(unsigned u) { return __builtin_bit_cast(float, u); }
DEVI unsigned f2u(float f) { return __builtin_bit_cast(unsigned, f); }
DEVI bool wave_all(bool p) { return __builtin_amdgcn_ballot_w64(!p) == 0ull; }

constexpr int RSRC_W3 = 0x00020000;
constexpr int SC1 = 16;
constexpr int EP_OFF = 0;
constexpr int FL_OFF = 4096;
constexpr int SG_OFF = 512 << 10;
constexpr int ST_OFF = 640 << 10;
constexpr int KF_OFF = 1 << 20;
constexpr int VF_OFF = 3 << 20;
constexpr int OF_OFF = 6 << 20;
constexpr int HF_OFF = 8 << 20;
constexpr int GF_OFF = 10 << 20;
constexpr int WS_BYTES = 17 << 20;

DEVI rsrc_t mk_rsrc(const void* p, int bytes) { return __builtin_amdgcn_make_buffer_rsrc((void*)p, 0, bytes, RSRC_W3); }
DEVI v4u mk_srd(const void* p, int bytes) { const u64 a = (u64)p; return v4u{(unsigned)a, (unsigned)(a >> 32) & 0xffffu, (unsigned)bytes, (unsigned)RSRC_W3}; }
DEVI f32x16 mfma32(v4u a, v4u b, f32x16 c) {
  return __builtin_amdgcn_mfma_f32_32x32x16_bf16(__builtin_bit_cast(bf16x8, a), __builtin_bit_cast(bf16x8, b), c, 0, 0, 0);
}
DEVI f32x4 mfma16(v4u a, v4u b, f32x4 c) {
  return __builtin_amdgcn_mfma_f32_16x16x32_bf16(__builtin_bit_cast(bf16x8, a), __builtin_bit_cast(bf16x8, b), c, 0, 0, 0);
}
DEVI f32x4 mfma4(unsigned a0, unsigned a1, unsigned b0, unsigned b1, f32x4 c) {
  return __builtin_amdgcn_mfma_f32_4x4x4bf16_1k(__builtin_bit_cast(s16x4, v2u{a0, a1}), __builtin_bit_cast(s16x4, v2u{b0, b1}), c, 0, 0, 0);
}
DEVI float swap32_add(float a, float b) {
  auto r = __builtin_amdgcn_permlane32_swap(f2u(a), f2u(b), false, false);
  return u2f(r[0]) + u2f(r[1]);
}
DEVI float swap16_add(float a) {
  auto r = __builtin_amdgcn_permlane16_swap(f2u(a), f2u(a), false, false);
  return u2f(r[0]) + u2f(r[1]);
}
template <int C> DEVI float dpp_add(float a) {
  return a + u2f(__builtin_amdgcn_update_dpp(0u, f2u(a), C, 0xf, 0xf, true));
}
DEVI unsigned pk2(float a, float b) { f32x2 f = {a, b}; return __builtin_bit_cast(unsigned, __builtin_convertvector(f, bf16x2)); }
DEVI float dot2(unsigned a, unsigned b, float c) { return __builtin_amdgcn_fdot2_f32_bf16(__builtin_bit_cast(bf16x2, a), __builtin_bit_cast(bf16x2, b), c, false); }
DEVI v2u trread(LAS char* p) { return __builtin_bit_cast(v2u, __builtin_amdgcn_ds_read_tr16_b64_v4i16((LAS s16x4*)p)); }
DEVI v4u trfrag(LAS char* p, int dt) { v2u a = trread(p), b = trread(p + dt); return v4u{a.x, a.y, b.x, b.y}; }
DEVI void dma16(v4u srd, unsigned lds, int voff, int soff) {
  asm volatile("s_mov_b32 m0, %0\n\ts_nop 0\n\tbuffer_load_dwordx4 %1, %2, %3 offen lds" ::"s"(lds), "v"(voff), "s"(srd), "s"(soff) : "memory");
}
DEVI v4u ld128(rsrc_t r, int v, int s) { return __builtin_amdgcn_raw_buffer_load_b128(r, v, s, 0); }
DEVI v4u ld128c(rsrc_t r, int v, int s) { return __builtin_amdgcn_raw_buffer_load_b128(r, v, s, SC1); }
DEVI v2u ld64c(rsrc_t r, int v, int s) { return __builtin_amdgcn_raw_buffer_load_b64(r, v, s, SC1); }
DEVI void st128c(v4u d, rsrc_t r, int v, int s) { __builtin_amdgcn_raw_buffer_store_b128(d, r, v + s, 0, SC1); }
DEVI void st64c(v2u d, rsrc_t r, int v, int s) { __builtin_amdgcn_raw_buffer_store_b64(d, r, v, s, SC1); }
DEVI LAS char* opq(LAS char* p) { asm("" : "+v"(p)); return p; }
DEVI float bflo(unsigned u) { return u2f(u << 16); }
DEVI float bfhi(unsigned u) { return u2f(u & 0xffff0000u); }
#define WAITV(n) asm volatile("s_waitcnt vmcnt(" #n ")" ::: "memory")
#define WAITL(n) asm volatile("s_waitcnt lgkmcnt(" #n ")" ::: "memory")
#define MEMBAR() asm volatile("" ::: "memory")
#define BAR() do { MEMBAR(); __builtin_amdgcn_s_barrier(); MEMBAR(); } while (0)
#define LBAR() do { WAITL(0); BAR(); } while (0)
#define SCHED() __builtin_amdgcn_sched_barrier(0)
constexpr unsigned ONES2 = 0x3f803f80u;

DEVI f32x16 smf(v4u a, f32x8 b, f32x16 c, int idx) {
  return __builtin_amdgcn_smfmac_f32_32x32x32_bf16(__builtin_bit_cast(bf16x8, a), __builtin_bit_cast(bf16x16, b), c, idx, 0, 0);
}
DEVI f32x8 lo8(f32x16 s) { return __builtin_shufflevector(s, s, 0, 1, 2, 3, 4, 5, 6, 7); }
DEVI f32x8 hi8(f32x16 s) { return __builtin_shufflevector(s, s, 8, 9, 10, 11, 12, 13, 14, 15); }

DEVI unsigned lds_inc(LAS unsigned* p, int) {
  unsigned old;
  asm volatile("s_mov_b64 exec, 1\n\tds_add_rtn_u32 %0, %1, %2\n\ts_mov_b64 exec, -1\n\ts_waitcnt lgkmcnt(0)" : "=&v"(old) : "v"((unsigned)(size_t)p), "v"(1u) : "memory");
  return __builtin_amdgcn_readfirstlane(old);
}
DEVI void lds_post(LAS unsigned* p, int) {
  asm volatile("s_mov_b64 exec, 1\n\tds_add_u32 %0, %1\n\ts_mov_b64 exec, -1" ::"v"((unsigned)(size_t)p), "v"(1u) : "memory");
}
DEVI void lds_wait(LAS unsigned* p, unsigned n) {
  unsigned t, left;
  asm volatile("ds_read_b32 %0, %2\n\ts_mov_b32 %1, 0x400000\n"
               "1:\n\ts_waitcnt lgkmcnt(0)\n\tv_cmp_le_u32 vcc, %3, %0\n\ts_cbranch_vccnz 2f\n\t"
               "s_sleep 2\n\tds_read_b32 %0, %2\n\ts_sub_u32 %1, %1, 1\n\ts_cbranch_scc0 1b\n\ts_waitcnt lgkmcnt(0)\n2:"
               : "=&v"(t), "=&s"(left) : "v"((unsigned)(size_t)p), "s"(n) : "memory", "vcc", "scc");
}

struct Flag { v4u d; int o; };
DEVI Flag flag_prep(int kind, int a, int b, u64 tag, int l) {
  const unsigned lo = (unsigned)tag, hi = (unsigned)(tag >> 32);
  Flag f = {v4u{lo, hi, lo, hi}, l < 8 ? FL_OFF + ((kind * 16 + a) * 16 + b) * 128 + (l & 7) * 16 : 0x40000000};
  asm volatile("" : "+v"(f.d), "+v"(f.o));
  return f;
}
DEVI GAS u64* flag_ptr(char* ws, int kind, int a, int l) { return (GAS u64*)(ws + FL_OFF + ((kind * 16 + a) * 16 + (l & 15)) * 128); }
DEVI void flag_wait(GAS u64* p, u64 tag) {
  u64 f; unsigned left;
  asm volatile("s_mov_b32 %1, 0x40000\n"
               "1:\n\tglobal_load_dwordx2 %0, %2, off sc1\n\ts_sub_u32 %1, %1, 1\n\ts_waitcnt vmcnt(0)\n\tv_cmp_ne_u64 vcc, %3, %0\n\t"
               "s_cbranch_vccz 2f\n\ts_sleep 8\n\ts_cbranch_scc0 1b\n2:"
               : "=&v"(f), "=&s"(left) : "v"(p), "s"(tag) : "memory", "vcc", "scc");
}
DEVI GAS u64* pin(GAS u64* p) { asm volatile("" : "+v"(p)); return p; }
DEVI int pin(int v) { asm volatile("" : "+s"(v)); return v; }

#define X2(a, b) "v_exp_f32 v" #a ", v" #a "\n\tv_exp_f32 v" #b ", v" #b "\n\t"
#define ATTN_LOOP_ASM \
  "s_nop 2\n\t" \
  "s_mov_b32 s92, 0xffff\n\t" \
  "s_mov_b32 s93, 0xffff\n\t" \
  "buffer_load_dwordx4 v[112:115], v160, s[64:67], 0 offen offset:0 sc1\n\t" \
  "v_mov_b64 v[128:129], v[166:167]\n\t" \
  "v_mov_b64 v[130:131], v[166:167]\n\t" \
  "v_mov_b64 v[132:133], v[166:167]\n\t" \
  "v_mov_b64 v[134:135], v[166:167]\n\t" \
  "s_mov_b64 exec, s[92:93]\n\t" \
  "buffer_load_dwordx4 v[128:131], v161, s[68:71], 0 offen offset:0 sc1\n\t" \
  "buffer_load_dwordx4 v[132:135], v161, s[68:71], 0 offen offset:512 sc1\n\t" \
  "s_mov_b64 exec, -1\n\t" \
  "buffer_load_dwordx4 v[116:119], v160, s[64:67], 0 offen offset:1024 sc1\n\t" \
  "v_mov_b64 v[136:137], v[166:167]\n\t" \
  "v_mov_b64 v[138:139], v[166:167]\n\t" \
  "v_mov_b64 v[140:141], v[166:167]\n\t" \
  "v_mov_b64 v[142:143], v[166:167]\n\t" \
  "s_mov_b64 exec, s[92:93]\n\t" \
  "buffer_load_dwordx4 v[136:139], v161, s[68:71], 0 offen offset:1024 sc1\n\t" \
  "buffer_load_dwordx4 v[140:143], v161, s[68:71], 0 offen offset:1536 sc1\n\t" \
  "s_mov_b64 exec, -1\n\t" \
  "buffer_load_dwordx4 v[120:123], v160, s[64:67], 0 offen offset:2048 sc1\n\t" \
  "v_mov_b64 v[144:145], v[166:167]\n\t" \
  "v_mov_b64 v[146:147], v[166:167]\n\t" \
  "v_mov_b64 v[148:149], v[166:167]\n\t" \
  "v_mov_b64 v[150:151], v[166:167]\n\t" \
  "s_mov_b64 exec, s[92:93]\n\t" \
  "buffer_load_dwordx4 v[144:147], v161, s[68:71], 0 offen offset:2048 sc1\n\t" \
  "buffer_load_dwordx4 v[148:151], v161, s[68:71], 0 offen offset:2560 sc1\n\t" \
  "s_mov_b64 exec, -1\n\t" \
  "buffer_load_dwordx4 v[124:127], v160, s[64:67], 0 offen offset:3072 sc1\n\t" \
  "v_xor_b32 v65, 0xc00, v165\n\t" \
  "ds_read_b128 v[96:99], v65\n\t" \
  "v_xor_b32 v66, 0x800, v165\n\t" \
  "ds_read_b128 v[100:103], v66\n\t" \
  "v_xor_b32 v67, 0x400, v165\n\t" \
  "ds_read_b128 v[104:107], v67\n\t" \
  "ds_read_b128 v[108:111], v165\n\t" \
  "v_lshrrev_b32 v229, 2, v160\n\t" \
  "v_add_u32 v226, s84, v160\n\t" \
  "v_add_u32 v227, s85, v160\n\t" \
  "v_add_u32 v228, s87, v160\n\t" \
  "v_add_u32 v231, s87, v229\n\t" \
  "v_add_u32 v230, s85, v229\n\t" \
  "v_add_u32 v229, s84, v229\n\t" \
  "v_mov_b64 v[152:153], v[166:167]\n\t" \
  "v_mov_b64 v[154:155], v[166:167]\n\t" \
  "v_mov_b64 v[156:157], v[166:167]\n\t" \
  "v_mov_b64 v[158:159], v[166:167]\n\t" \
  "v_mov_b64 v[0:1], 0\n\t" \
  "v_mov_b64 v[2:3], 0\n\t" \
  "v_mov_b64 v[4:5], 0\n\t" \
  "v_mov_b64 v[6:7], 0\n\t" \
  "v_mov_b64 v[8:9], 0\n\t" \
  "v_mov_b64 v[16:17], 0\n\t" \
  "v_mov_b64 v[18:19], 0\n\t" \
  "v_mov_b64 v[20:21], 0\n\t" \
  "v_mov_b64 v[22:23], 0\n\t" \
  "v_mov_b64 v[24:25], 0\n\t" \
  "v_mov_b64 v[32:33], 0\n\t" \
  "v_mov_b64 v[34:35], 0\n\t" \
  "v_mov_b64 v[36:37], 0\n\t" \
  "v_mov_b64 v[38:39], 0\n\t" \
  "v_mov_b64 v[40:41], 0\n\t" \
  "v_mov_b64 v[48:49], 0\n\t" \
  "v_mov_b64 v[50:51], 0\n\t" \
  "v_mov_b64 v[52:53], 0\n\t" \
  "v_mov_b64 v[54:55], 0\n\t" \
  "v_mov_b64 v[56:57], 0\n\t" \
  "s_movk_i32 s88, 0x1000\n\t" \
  "s_movk_i32 s89, 0x2000\n\t" \
  "s_movk_i32 s90, 0x3000\n\t" \
  "s_setprio 3\n\t" \
  "s_waitcnt lgkmcnt(0)\n\t" \
  "s_waitcnt vmcnt(9)\n\t" \
  "v_mfma_f32_32x32x16_bf16 v[64:79], v[112:115], v[96:99], 0\n\t" \
  "s_nop 12\n\t" \
  X2(64, 65) "s_mov_b64 exec, s[92:93]\n\t" \
  "buffer_load_dwordx4 v[152:155], v161, s[68:71], 0 offen offset:3072 sc1\n\t" \
  "buffer_load_dwordx4 v[156:159], v161, s[68:71], 0 offen offset:3584 sc1\n\t" \
  "s_mov_b64 exec, -1\n\t" \
  X2(66, 67) "v_mfma_f32_32x32x16_bf16 v[80:95], v[112:115], v[100:103], 0\n\t" \
  X2(68, 69) X2(70, 71) X2(72, 73) "s_waitcnt vmcnt(9)\n\t" \
  "v_smfmac_f32_32x32x32_bf16 v[0:15], v[128:131], v[64:71], v162\n\t" \
  X2(74, 75) X2(76, 77) X2(78, 79) X2(80, 81) "v_smfmac_f32_32x32x32_bf16 v[0:15], v[132:135], v[72:79], v162\n\t" \
  X2(82, 83) "v_mfma_f32_32x32x16_bf16 v[64:79], v[112:115], v[104:107], 0\n\t" \
  X2(84, 85) X2(86, 87) X2(88, 89) "v_smfmac_f32_32x32x32_bf16 v[16:31], v[128:131], v[80:87], v162\n\t" \
  X2(90, 91) X2(92, 93) X2(94, 95) X2(64, 65) "v_smfmac_f32_32x32x32_bf16 v[16:31], v[132:135], v[88:95], v162\n\t" \
  X2(66, 67) "v_mfma_f32_32x32x16_bf16 v[80:95], v[112:115], v[108:111], 0\n\t" \
  "buffer_load_dwordx4 v[112:115], v160, s[64:67], s88 offen offset:0 sc1\n\t" \
  X2(68, 69) X2(70, 71) X2(72, 73) "v_smfmac_f32_32x32x32_bf16 v[32:47], v[128:131], v[64:71], v162\n\t" \
  X2(74, 75) X2(76, 77) X2(78, 79) X2(80, 81) "v_smfmac_f32_32x32x32_bf16 v[32:47], v[132:135], v[72:79], v162\n\t" \
  X2(82, 83) "s_waitcnt vmcnt(9)\n\t" \
  "v_mfma_f32_32x32x16_bf16 v[64:79], v[116:119], v[96:99], 0\n\t" \
  X2(84, 85) X2(86, 87) X2(88, 89) "v_smfmac_f32_32x32x32_bf16 v[48:63], v[128:131], v[80:87], v162\n\t" \
  X2(90, 91) X2(92, 93) X2(94, 95) X2(64, 65) "v_smfmac_f32_32x32x32_bf16 v[48:63], v[132:135], v[88:95], v162\n\t" \
  "s_mov_b64 exec, s[92:93]\n\t" \
  "buffer_load_dwordx4 v[128:131], v161, s[68:71], s88 offen offset:0 sc1\n\t" \
  "buffer_load_dwordx4 v[132:135], v161, s[68:71], s88 offen offset:512 sc1\n\t" \
  "s_mov_b64 exec, -1\n\t" \
  X2(66, 67) "v_mfma_f32_32x32x16_bf16 v[80:95], v[116:119], v[100:103], 0\n\t" \
  X2(68, 69) X2(70, 71) X2(72, 73) "s_waitcnt vmcnt(9)\n\t" \
  "v_smfmac_f32_32x32x32_bf16 v[0:15], v[136:139], v[64:71], v162\n\t" \
  X2(74, 75) X2(76, 77) X2(78, 79) X2(80, 81) "v_smfmac_f32_32x32x32_bf16 v[0:15], v[140:143], v[72:79], v162\n\t" \
  X2(82, 83) "v_mfma_f32_32x32x16_bf16 v[64:79], v[116:119], v[104:107], 0\n\t" \
  X2(84, 85) X2(86, 87) X2(88, 89) "v_smfmac_f32_32x32x32_bf16 v[16:31], v[136:139], v[80:87], v162\n\t" \
  X2(90, 91) X2(92, 93) X2(94, 95) X2(64, 65) "v_smfmac_f32_32x32x32_bf16 v[16:31], v[140:143], v[88:95], v162\n\t" \
  X2(66, 67) "v_mfma_f32_32x32x16_bf16 v[80:95], v[116:119], v[108:111], 0\n\t" \
  "buffer_load_dwordx4 v[116:119], v160, s[64:67], s88 offen offset:1024 sc1\n\t" \
  X2(68, 69) X2(70, 71) X2(72, 73) "v_smfmac_f32_32x32x32_bf16 v[32:47], v[136:139], v[64:71], v162\n\t" \
  X2(74, 75) X2(76, 77) X2(78, 79) X2(80, 81) "v_smfmac_f32_32x32x32_bf16 v[32:47], v[140:143], v[72:79], v162\n\t" \
  X2(82, 83) "s_waitcnt vmcnt(9)\n\t" \
  "v_mfma_f32_32x32x16_bf16 v[64:79], v[120:123], v[96:99], 0\n\t" \
  X2(84, 85) X2(86, 87) X2(88, 89) "v_smfmac_f32_32x32x32_bf16 v[48:63], v[136:139], v[80:87], v162\n\t" \
  X2(90, 91) X2(92, 93) X2(94, 95) X2(64, 65) "v_smfmac_f32_32x32x32_bf16 v[48:63], v[140:143], v[88:95], v162\n\t" \
  "s_mov_b64 exec, s[92:93]\n\t" \
  "buffer_load_dwordx4 v[136:139], v161, s[68:71], s88 offen offset:1024 sc1\n\t" \
  "buffer_load_dwordx4 v[140:143], v161, s[68:71], s88 offen offset:1536 sc1\n\t" \
  "s_mov_b64 exec, -1\n\t" \
  X2(66, 67) "v_mfma_f32_32x32x16_bf16 v[80:95], v[120:123], v[100:103], 0\n\t" \
  X2(68, 69) X2(70, 71) X2(72, 73) "s_waitcnt vmcnt(9)\n\t" \
  "v_smfmac_f32_32x32x32_bf16 v[0:15], v[144:147], v[64:71], v162\n\t" \
  X2(74, 75) X2(76, 77) X2(78, 79) X2(80, 81) "v_smfmac_f32_32x32x32_bf16 v[0:15], v[148:151], v[72:79], v162\n\t" \
  "s_mov_b32 m0, s81\n\t" \
  "s_add_u32 s81, s81, s83\n\t" \
  "buffer_load_dwordx4 v163, s[72:75], s80 offen lds\n\t" \
  "s_add_u32 s80, s80, s82\n\t" \
  X2(82, 83) "v_mfma_f32_32x32x16_bf16 v[64:79], v[120:123], v[104:107], 0\n\t" \
  X2(84, 85) X2(86, 87) X2(88, 89) "v_smfmac_f32_32x32x32_bf16 v[16:31], v[144:147], v[80:87], v162\n\t" \
  X2(90, 91) X2(92, 93) X2(94, 95) X2(64, 65) "v_smfmac_f32_32x32x32_bf16 v[16:31], v[148:151], v[88:95], v162\n\t" \
  X2(66, 67) "v_mfma_f32_32x32x16_bf16 v[80:95], v[120:123], v[108:111], 0\n\t" \
  "buffer_load_dwordx4 v[120:123], v160, s[64:67], s88 offen offset:2048 sc1\n\t" \
  X2(68, 69) X2(70, 71) X2(72, 73) "v_smfmac_f32_32x32x32_bf16 v[32:47], v[144:147], v[64:71], v162\n\t" \
  X2(74, 75) X2(76, 77) X2(78, 79) X2(80, 81) "v_smfmac_f32_32x32x32_bf16 v[32:47], v[148:151], v[72:79], v162\n\t" \
  "global_load_dword v217, v[222:223], off\n\t" \
  X2(82, 83) "s_waitcnt vmcnt(11)\n\t" \
  "v_mfma_f32_32x32x16_bf16 v[64:79], v[124:127], v[96:99], 0\n\t" \
  X2(84, 85) X2(86, 87) X2(88, 89) "v_smfmac_f32_32x32x32_bf16 v[48:63], v[144:147], v[80:87], v162\n\t" \
  X2(90, 91) X2(92, 93) X2(94, 95) X2(64, 65) "v_smfmac_f32_32x32x32_bf16 v[48:63], v[148:151], v[88:95], v162\n\t" \
  "s_mov_b64 exec, s[92:93]\n\t" \
  "buffer_load_dwordx4 v[144:147], v161, s[68:71], s88 offen offset:2048 sc1\n\t" \
  "buffer_load_dwordx4 v[148:151], v161, s[68:71], s88 offen offset:2560 sc1\n\t" \
  "s_mov_b64 exec, -1\n\t" \
  X2(66, 67) "v_mfma_f32_32x32x16_bf16 v[80:95], v[124:127], v[100:103], 0\n\t" \
  X2(68, 69) X2(70, 71) X2(72, 73) "s_waitcnt vmcnt(11)\n\t" \
  "v_smfmac_f32_32x32x32_bf16 v[0:15], v[152:155], v[64:71], v162\n\t" \
  X2(74, 75) X2(76, 77) X2(78, 79) X2(80, 81) "v_smfmac_f32_32x32x32_bf16 v[0:15], v[156:159], v[72:79], v162\n\t" \
  "s_mov_b32 m0, s81\n\t" \
  "s_add_u32 s81, s81, s83\n\t" \
  "buffer_load_dwordx4 v163, s[72:75], s80 offen lds\n\t" \
  "s_add_u32 s80, s80, s82\n\t" \
  X2(82, 83) "v_mfma_f32_32x32x16_bf16 v[64:79], v[124:127], v[104:107], 0\n\t" \
  X2(84, 85) X2(86, 87) X2(88, 89) "v_smfmac_f32_32x32x32_bf16 v[16:31], v[152:155], v[80:87], v162\n\t" \
  X2(90, 91) X2(92, 93) X2(94, 95) X2(64, 65) "v_smfmac_f32_32x32x32_bf16 v[16:31], v[156:159], v[88:95], v162\n\t" \
  X2(66, 67) "v_mfma_f32_32x32x16_bf16 v[80:95], v[124:127], v[108:111], 0\n\t" \
  "buffer_load_dwordx4 v[124:127], v160, s[64:67], s88 offen offset:3072 sc1\n\t" \
  X2(68, 69) X2(70, 71) X2(72, 73) "v_smfmac_f32_32x32x32_bf16 v[32:47], v[152:155], v[64:71], v162\n\t" \
  X2(74, 75) X2(76, 77) X2(78, 79) X2(80, 81) "v_smfmac_f32_32x32x32_bf16 v[32:47], v[156:159], v[72:79], v162\n\t" \
  X2(82, 83) "s_waitcnt vmcnt(12)\n\t" \
  "v_mfma_f32_32x32x16_bf16 v[64:79], v[112:115], v[96:99], 0\n\t" \
  X2(84, 85) X2(86, 87) X2(88, 89) "v_smfmac_f32_32x32x32_bf16 v[48:63], v[152:155], v[80:87], v162\n\t" \
  X2(90, 91) X2(92, 93) X2(94, 95) X2(64, 65) "v_smfmac_f32_32x32x32_bf16 v[48:63], v[156:159], v[88:95], v162\n\t" \
  "s_mov_b64 exec, s[92:93]\n\t" \
  "buffer_load_dwordx4 v[152:155], v161, s[68:71], s88 offen offset:3072 sc1\n\t" \
  "buffer_load_dwordx4 v[156:159], v161, s[68:71], s88 offen offset:3584 sc1\n\t" \
  "s_mov_b64 exec, -1\n\t" \
  X2(66, 67) "v_mfma_f32_32x32x16_bf16 v[80:95], v[112:115], v[100:103], 0\n\t" \
  X2(68, 69) X2(70, 71) X2(72, 73) "s_waitcnt vmcnt(12)\n\t" \
  "v_smfmac_f32_32x32x32_bf16 v[0:15], v[128:131], v[64:71], v162\n\t" \
  X2(74, 75) X2(76, 77) X2(78, 79) X2(80, 81) "v_smfmac_f32_32x32x32_bf16 v[0:15], v[132:135], v[72:79], v162\n\t" \
  "s_mov_b32 m0, s81\n\t" \
  "s_add_u32 s81, s81, s83\n\t" \
  "buffer_load_dwordx4 v163, s[72:75], s80 offen lds\n\t" \
  "s_add_u32 s80, s80, s82\n\t" \
  X2(82, 83) "v_mfma_f32_32x32x16_bf16 v[64:79], v[112:115], v[104:107], 0\n\t" \
  X2(84, 85) X2(86, 87) X2(88, 89) "v_smfmac_f32_32x32x32_bf16 v[16:31], v[128:131], v[80:87], v162\n\t" \
  X2(90, 91) X2(92, 93) X2(94, 95) X2(64, 65) "v_smfmac_f32_32x32x32_bf16 v[16:31], v[132:135], v[88:95], v162\n\t" \
  X2(66, 67) "v_mfma_f32_32x32x16_bf16 v[80:95], v[112:115], v[108:111], 0\n\t" \
  "buffer_load_dwordx4 v[112:115], v160, s[64:67], s89 offen offset:0 sc1\n\t" \
  X2(68, 69) X2(70, 71) X2(72, 73) "v_smfmac_f32_32x32x32_bf16 v[32:47], v[128:131], v[64:71], v162\n\t" \
  X2(74, 75) X2(76, 77) X2(78, 79) X2(80, 81) "v_smfmac_f32_32x32x32_bf16 v[32:47], v[132:135], v[72:79], v162\n\t" \
  X2(82, 83) "s_waitcnt vmcnt(13)\n\t" \
  "v_mfma_f32_32x32x16_bf16 v[64:79], v[116:119], v[96:99], 0\n\t" \
  X2(84, 85) X2(86, 87) X2(88, 89) "v_smfmac_f32_32x32x32_bf16 v[48:63], v[128:131], v[80:87], v162\n\t" \
  X2(90, 91) X2(92, 93) X2(94, 95) X2(64, 65) "v_smfmac_f32_32x32x32_bf16 v[48:63], v[132:135], v[88:95], v162\n\t" \
  "s_mov_b64 exec, s[92:93]\n\t" \
  "buffer_load_dwordx4 v[128:131], v161, s[68:71], s89 offen offset:0 sc1\n\t" \
  "buffer_load_dwordx4 v[132:135], v161, s[68:71], s89 offen offset:512 sc1\n\t" \
  "s_mov_b64 exec, -1\n\t" \
  X2(66, 67) "v_mfma_f32_32x32x16_bf16 v[80:95], v[116:119], v[100:103], 0\n\t" \
  X2(68, 69) X2(70, 71) X2(72, 73) "s_waitcnt vmcnt(13)\n\t" \
  "v_smfmac_f32_32x32x32_bf16 v[0:15], v[136:139], v[64:71], v162\n\t" \
  X2(74, 75) X2(76, 77) X2(78, 79) X2(80, 81) "v_smfmac_f32_32x32x32_bf16 v[0:15], v[140:143], v[72:79], v162\n\t" \
  "s_mov_b32 m0, s81\n\t" \
  "s_add_u32 s81, s81, s83\n\t" \
  "buffer_load_dwordx4 v163, s[72:75], s80 offen lds\n\t" \
  "s_add_u32 s80, s80, s82\n\t" \
  X2(82, 83) "v_mfma_f32_32x32x16_bf16 v[64:79], v[116:119], v[104:107], 0\n\t" \
  X2(84, 85) X2(86, 87) X2(88, 89) "v_smfmac_f32_32x32x32_bf16 v[16:31], v[136:139], v[80:87], v162\n\t" \
  X2(90, 91) X2(92, 93) X2(94, 95) X2(64, 65) "v_smfmac_f32_32x32x32_bf16 v[16:31], v[140:143], v[88:95], v162\n\t" \
  X2(66, 67) "v_mfma_f32_32x32x16_bf16 v[80:95], v[116:119], v[108:111], 0\n\t" \
  "buffer_load_dwordx4 v[116:119], v160, s[64:67], s89 offen offset:1024 sc1\n\t" \
  X2(68, 69) X2(70, 71) X2(72, 73) "v_smfmac_f32_32x32x32_bf16 v[32:47], v[136:139], v[64:71], v162\n\t" \
  X2(74, 75) X2(76, 77) X2(78, 79) X2(80, 81) "v_smfmac_f32_32x32x32_bf16 v[32:47], v[140:143], v[72:79], v162\n\t" \
  "buffer_load_dwordx4 v[168:171], v164, s[76:79], s86 offen\n\t" \
  "s_add_u32 s86, s86, 0x2000\n\t" \
  X2(82, 83) "s_waitcnt vmcnt(14)\n\t" \
  "v_mfma_f32_32x32x16_bf16 v[64:79], v[120:123], v[96:99], 0\n\t" \
  X2(84, 85) X2(86, 87) X2(88, 89) "v_smfmac_f32_32x32x32_bf16 v[48:63], v[136:139], v[80:87], v162\n\t" \
  X2(90, 91) X2(92, 93) X2(94, 95) X2(64, 65) "v_smfmac_f32_32x32x32_bf16 v[48:63], v[140:143], v[88:95], v162\n\t" \
  "s_mov_b64 exec, s[92:93]\n\t" \
  "buffer_load_dwordx4 v[136:139], v161, s[68:71], s89 offen offset:1024 sc1\n\t" \
  "buffer_load_dwordx4 v[140:143], v161, s[68:71], s89 offen offset:1536 sc1\n\t" \
  "s_mov_b64 exec, -1\n\t" \
  X2(66, 67) "v_mfma_f32_32x32x16_bf16 v[80:95], v[120:123], v[100:103], 0\n\t" \
  X2(68, 69) X2(70, 71) X2(72, 73) "s_waitcnt vmcnt(13)\n\t" \
  "v_smfmac_f32_32x32x32_bf16 v[0:15], v[144:147], v[64:71], v162\n\t" \
  X2(74, 75) X2(76, 77) X2(78, 79) X2(80, 81) "v_smfmac_f32_32x32x32_bf16 v[0:15], v[148:151], v[72:79], v162\n\t" \
  "s_mov_b32 m0, s81\n\t" \
  "s_add_u32 s81, s81, s83\n\t" \
  "buffer_load_dwordx4 v163, s[72:75], s80 offen lds\n\t" \
  "s_add_u32 s80, s80, s82\n\t" \
  X2(82, 83) "v_mfma_f32_32x32x16_bf16 v[64:79], v[120:123], v[104:107], 0\n\t" \
  X2(84, 85) X2(86, 87) X2(88, 89) "v_smfmac_f32_32x32x32_bf16 v[16:31], v[144:147], v[80:87], v162\n\t" \
  X2(90, 91) X2(92, 93) X2(94, 95) X2(64, 65) "v_smfmac_f32_32x32x32_bf16 v[16:31], v[148:151], v[88:95], v162\n\t" \
  X2(66, 67) "v_mfma_f32_32x32x16_bf16 v[80:95], v[120:123], v[108:111], 0\n\t" \
  "buffer_load_dwordx4 v[120:123], v160, s[64:67], s89 offen offset:2048 sc1\n\t" \
  X2(68, 69) X2(70, 71) X2(72, 73) "v_smfmac_f32_32x32x32_bf16 v[32:47], v[144:147], v[64:71], v162\n\t" \
  X2(74, 75) X2(76, 77) X2(78, 79) X2(80, 81) "v_smfmac_f32_32x32x32_bf16 v[32:47], v[148:151], v[72:79], v162\n\t" \
  "buffer_load_dwordx4 v[172:175], v164, s[76:79], s86 offen\n\t" \
  "s_add_u32 s86, s86, 0x2000\n\t" \
  X2(82, 83) "s_waitcnt vmcnt(14)\n\t" \
  "v_mfma_f32_32x32x16_bf16 v[64:79], v[124:127], v[96:99], 0\n\t" \
  X2(84, 85) X2(86, 87) X2(88, 89) "v_smfmac_f32_32x32x32_bf16 v[48:63], v[144:147], v[80:87], v162\n\t" \
  X2(90, 91) X2(92, 93) X2(94, 95) X2(64, 65) "v_smfmac_f32_32x32x32_bf16 v[48:63], v[148:151], v[88:95], v162\n\t" \
  "s_mov_b64 exec, s[92:93]\n\t" \
  "buffer_load_dwordx4 v[144:147], v161, s[68:71], s89 offen offset:2048 sc1\n\t" \
  "buffer_load_dwordx4 v[148:151], v161, s[68:71], s89 offen offset:2560 sc1\n\t" \
  "s_mov_b64 exec, -1\n\t" \
  X2(66, 67) "v_mfma_f32_32x32x16_bf16 v[80:95], v[124:127], v[100:103], 0\n\t" \
  X2(68, 69) X2(70, 71) X2(72, 73) "s_waitcnt vmcnt(14)\n\t" \
  "v_smfmac_f32_32x32x32_bf16 v[0:15], v[152:155], v[64:71], v162\n\t" \
  X2(74, 75) X2(76, 77) X2(78, 79) X2(80, 81) "v_smfmac_f32_32x32x32_bf16 v[0:15], v[156:159], v[72:79], v162\n\t" \
  "s_mov_b32 m0, s81\n\t" \
  "s_add_u32 s81, s81, s83\n\t" \
  "buffer_load_dwordx4 v163, s[72:75], s80 offen lds\n\t" \
  "s_add_u32 s80, s80, s82\n\t" \
  X2(82, 83) "v_mfma_f32_32x32x16_bf16 v[64:79], v[124:127], v[104:107], 0\n\t" \
  X2(84, 85) X2(86, 87) X2(88, 89) "v_smfmac_f32_32x32x32_bf16 v[16:31], v[152:155], v[80:87], v162\n\t" \
  X2(90, 91) X2(92, 93) X2(94, 95) X2(64, 65) "v_smfmac_f32_32x32x32_bf16 v[16:31], v[156:159], v[88:95], v162\n\t" \
  X2(66, 67) "v_mfma_f32_32x32x16_bf16 v[80:95], v[124:127], v[108:111], 0\n\t" \
  "buffer_load_dwordx4 v[124:127], v160, s[64:67], s89 offen offset:3072 sc1\n\t" \
  X2(68, 69) X2(70, 71) X2(72, 73) "v_smfmac_f32_32x32x32_bf16 v[32:47], v[152:155], v[64:71], v162\n\t" \
  X2(74, 75) X2(76, 77) X2(78, 79) X2(80, 81) "v_smfmac_f32_32x32x32_bf16 v[32:47], v[156:159], v[72:79], v162\n\t" \
  "buffer_load_dwordx4 v[176:179], v164, s[76:79], s86 offen\n\t" \
  "s_add_u32 s86, s86, 0x2000\n\t" \
  X2(82, 83) "s_waitcnt vmcnt(15)\n\t" \
  "v_mfma_f32_32x32x16_bf16 v[64:79], v[112:115], v[96:99], 0\n\t" \
  X2(84, 85) X2(86, 87) X2(88, 89) "v_smfmac_f32_32x32x32_bf16 v[48:63], v[152:155], v[80:87], v162\n\t" \
  X2(90, 91) X2(92, 93) X2(94, 95) X2(64, 65) "v_smfmac_f32_32x32x32_bf16 v[48:63], v[156:159], v[88:95], v162\n\t" \
  "s_mov_b64 exec, s[92:93]\n\t" \
  "buffer_load_dwordx4 v[152:155], v161, s[68:71], s89 offen offset:3072 sc1\n\t" \
  "buffer_load_dwordx4 v[156:159], v161, s[68:71], s89 offen offset:3584 sc1\n\t" \
  "s_mov_b64 exec, -1\n\t" \
  X2(66, 67) "v_mfma_f32_32x32x16_bf16 v[80:95], v[112:115], v[100:103], 0\n\t" \
  X2(68, 69) X2(70, 71) X2(72, 73) "s_waitcnt vmcnt(15)\n\t" \
  "v_smfmac_f32_32x32x32_bf16 v[0:15], v[128:131], v[64:71], v162\n\t" \
  "s_setprio 2\n\t" \
  X2(74, 75) X2(76, 77) X2(78, 79) X2(80, 81) "v_smfmac_f32_32x32x32_bf16 v[0:15], v[132:135], v[72:79], v162\n\t" \
  "s_mov_b32 m0, s81\n\t" \
  "s_add_u32 s81, s81, s83\n\t" \
  "buffer_load_dwordx4 v163, s[72:75], s80 offen lds\n\t" \
  "s_add_u32 s80, s80, s82\n\t" \
  X2(82, 83) "v_mfma_f32_32x32x16_bf16 v[64:79], v[112:115], v[104:107], 0\n\t" \
  X2(84, 85) X2(86, 87) X2(88, 89) "v_smfmac_f32_32x32x32_bf16 v[16:31], v[128:131], v[80:87], v162\n\t" \
  X2(90, 91) X2(92, 93) X2(94, 95) X2(64, 65) "v_smfmac_f32_32x32x32_bf16 v[16:31], v[132:135], v[88:95], v162\n\t" \
  X2(66, 67) "v_mfma_f32_32x32x16_bf16 v[80:95], v[112:115], v[108:111], 0\n\t" \
  "buffer_load_dwordx4 v[112:115], v160, s[64:67], s90 offen offset:0 sc1\n\t" \
  X2(68, 69) X2(70, 71) X2(72, 73) "v_smfmac_f32_32x32x32_bf16 v[32:47], v[128:131], v[64:71], v162\n\t" \
  X2(74, 75) X2(76, 77) X2(78, 79) X2(80, 81) "v_smfmac_f32_32x32x32_bf16 v[32:47], v[132:135], v[72:79], v162\n\t" \
  "buffer_load_dwordx4 v[180:183], v164, s[76:79], s86 offen\n\t" \
  "s_add_u32 s86, s86, 0x2000\n\t" \
  X2(82, 83) "s_waitcnt vmcnt(16)\n\t" \
  "v_mfma_f32_32x32x16_bf16 v[64:79], v[116:119], v[96:99], 0\n\t" \
  X2(84, 85) X2(86, 87) X2(88, 89) "v_smfmac_f32_32x32x32_bf16 v[48:63], v[128:131], v[80:87], v162\n\t" \
  X2(90, 91) X2(92, 93) X2(94, 95) X2(64, 65) "v_smfmac_f32_32x32x32_bf16 v[48:63], v[132:135], v[88:95], v162\n\t" \
  "s_mov_b64 exec, s[92:93]\n\t" \
  "buffer_load_dwordx4 v[128:131], v161, s[68:71], s90 offen offset:0 sc1\n\t" \
  "buffer_load_dwordx4 v[132:135], v161, s[68:71], s90 offen offset:512 sc1\n\t" \
  "s_mov_b64 exec, -1\n\t" \
  X2(66, 67) "v_mfma_f32_32x32x16_bf16 v[80:95], v[116:119], v[100:103], 0\n\t" \
  X2(68, 69) X2(70, 71) X2(72, 73) "s_waitcnt vmcnt(15)\n\t" \
  "v_smfmac_f32_32x32x32_bf16 v[0:15], v[136:139], v[64:71], v162\n\t" \
  X2(74, 75) X2(76, 77) X2(78, 79) X2(80, 81) "v_smfmac_f32_32x32x32_bf16 v[0:15], v[140:143], v[72:79], v162\n\t" \
  "s_mov_b32 m0, s81\n\t" \
  "s_add_u32 s81, s81, s83\n\t" \
  "buffer_load_dwordx4 v163, s[72:75], s80 offen lds\n\t" \
  "s_add_u32 s80, s80, s82\n\t" \
  X2(82, 83) "v_mfma_f32_32x32x16_bf16 v[64:79], v[116:119], v[104:107], 0\n\t" \
  X2(84, 85) X2(86, 87) X2(88, 89) "v_smfmac_f32_32x32x32_bf16 v[16:31], v[136:139], v[80:87], v162\n\t" \
  X2(90, 91) X2(92, 93) X2(94, 95) X2(64, 65) "v_smfmac_f32_32x32x32_bf16 v[16:31], v[140:143], v[88:95], v162\n\t" \
  X2(66, 67) "v_mfma_f32_32x32x16_bf16 v[80:95], v[116:119], v[108:111], 0\n\t" \
  "buffer_load_dwordx4 v[116:119], v160, s[64:67], s90 offen offset:1024 sc1\n\t" \
  X2(68, 69) X2(70, 71) X2(72, 73) "v_smfmac_f32_32x32x32_bf16 v[32:47], v[136:139], v[64:71], v162\n\t" \
  X2(74, 75) X2(76, 77) X2(78, 79) X2(80, 81) "v_smfmac_f32_32x32x32_bf16 v[32:47], v[140:143], v[72:79], v162\n\t" \
  "buffer_load_dwordx4 v[184:187], v164, s[76:79], s86 offen\n\t" \
  "s_add_u32 s86, s86, 0x2000\n\t" \
  X2(82, 83) "s_waitcnt vmcnt(16)\n\t" \
  "v_mfma_f32_32x32x16_bf16 v[64:79], v[120:123], v[96:99], 0\n\t" \
  X2(84, 85) X2(86, 87) X2(88, 89) "v_smfmac_f32_32x32x32_bf16 v[48:63], v[136:139], v[80:87], v162\n\t" \
  X2(90, 91) X2(92, 93) X2(94, 95) X2(64, 65) "v_smfmac_f32_32x32x32_bf16 v[48:63], v[140:143], v[88:95], v162\n\t" \
  "s_mov_b64 exec, s[92:93]\n\t" \
  "buffer_load_dwordx4 v[136:139], v161, s[68:71], s90 offen offset:1024 sc1\n\t" \
  "buffer_load_dwordx4 v[140:143], v161, s[68:71], s90 offen offset:1536 sc1\n\t" \
  "s_mov_b64 exec, -1\n\t" \
  X2(66, 67) "v_mfma_f32_32x32x16_bf16 v[80:95], v[120:123], v[100:103], 0\n\t" \
  X2(68, 69) X2(70, 71) X2(72, 73) "s_waitcnt vmcnt(15)\n\t" \
  "v_smfmac_f32_32x32x32_bf16 v[0:15], v[144:147], v[64:71], v162\n\t" \
  X2(74, 75) X2(76, 77) X2(78, 79) X2(80, 81) "v_smfmac_f32_32x32x32_bf16 v[0:15], v[148:151], v[72:79], v162\n\t" \
  "s_mov_b32 m0, s81\n\t" \
  "s_add_u32 s81, s81, s83\n\t" \
  "buffer_load_dwordx4 v163, s[72:75], s80 offen lds\n\t" \
  "s_add_u32 s80, s80, s82\n\t" \
  X2(82, 83) "v_mfma_f32_32x32x16_bf16 v[64:79], v[120:123], v[104:107], 0\n\t" \
  X2(84, 85) X2(86, 87) X2(88, 89) "v_smfmac_f32_32x32x32_bf16 v[16:31], v[144:147], v[80:87], v162\n\t" \
  X2(90, 91) X2(92, 93) X2(94, 95) X2(64, 65) "v_smfmac_f32_32x32x32_bf16 v[16:31], v[148:151], v[88:95], v162\n\t" \
  X2(66, 67) "v_mfma_f32_32x32x16_bf16 v[80:95], v[120:123], v[108:111], 0\n\t" \
  "buffer_load_dwordx4 v[120:123], v160, s[64:67], s90 offen offset:2048 sc1\n\t" \
  X2(68, 69) X2(70, 71) X2(72, 73) "v_smfmac_f32_32x32x32_bf16 v[32:47], v[144:147], v[64:71], v162\n\t" \
  X2(74, 75) X2(76, 77) X2(78, 79) X2(80, 81) "v_smfmac_f32_32x32x32_bf16 v[32:47], v[148:151], v[72:79], v162\n\t" \
  "buffer_load_dwordx4 v[188:191], v164, s[76:79], s86 offen\n\t" \
  "s_add_u32 s86, s86, 0x2000\n\t" \
  X2(82, 83) "s_waitcnt vmcnt(16)\n\t" \
  "v_mfma_f32_32x32x16_bf16 v[64:79], v[124:127], v[96:99], 0\n\t" \
  X2(84, 85) X2(86, 87) X2(88, 89) "v_smfmac_f32_32x32x32_bf16 v[48:63], v[144:147], v[80:87], v162\n\t" \
  X2(90, 91) X2(92, 93) X2(94, 95) X2(64, 65) "v_smfmac_f32_32x32x32_bf16 v[48:63], v[148:151], v[88:95], v162\n\t" \
  "s_mov_b64 exec, s[92:93]\n\t" \
  "buffer_load_dwordx4 v[144:147], v161, s[68:71], s90 offen offset:2048 sc1\n\t" \
  "buffer_load_dwordx4 v[148:151], v161, s[68:71], s90 offen offset:2560 sc1\n\t" \
  "s_mov_b64 exec, -1\n\t" \
  X2(66, 67) "v_mfma_f32_32x32x16_bf16 v[80:95], v[124:127], v[100:103], 0\n\t" \
  X2(68, 69) X2(70, 71) X2(72, 73) "s_waitcnt vmcnt(15)\n\t" \
  "v_smfmac_f32_32x32x32_bf16 v[0:15], v[152:155], v[64:71], v162\n\t" \
  X2(74, 75) X2(76, 77) X2(78, 79) X2(80, 81) "v_smfmac_f32_32x32x32_bf16 v[0:15], v[156:159], v[72:79], v162\n\t" \
  "s_mov_b32 m0, s81\n\t" \
  "s_add_u32 s81, s81, s83\n\t" \
  "buffer_load_dwordx4 v163, s[72:75], s80 offen lds\n\t" \
  "s_add_u32 s80, s80, s82\n\t" \
  X2(82, 83) "v_mfma_f32_32x32x16_bf16 v[64:79], v[124:127], v[104:107], 0\n\t" \
  X2(84, 85) X2(86, 87) X2(88, 89) "v_smfmac_f32_32x32x32_bf16 v[16:31], v[152:155], v[80:87], v162\n\t" \
  X2(90, 91) X2(92, 93) X2(94, 95) X2(64, 65) "v_smfmac_f32_32x32x32_bf16 v[16:31], v[156:159], v[88:95], v162\n\t" \
  "buffer_load_dwordx4 v[192:195], v164, s[76:79], s86 offen\n\t" \
  "s_add_u32 s86, s86, 0x2000\n\t" \
  X2(66, 67) "v_mfma_f32_32x32x16_bf16 v[80:95], v[124:127], v[108:111], 0\n\t" \
  "buffer_load_dwordx4 v[124:127], v160, s[64:67], s90 offen offset:3072 sc1\n\t" \
  X2(68, 69) X2(70, 71) X2(72, 73) "v_smfmac_f32_32x32x32_bf16 v[32:47], v[152:155], v[64:71], v162\n\t" \
  X2(74, 75) X2(76, 77) X2(78, 79) X2(80, 81) "v_smfmac_f32_32x32x32_bf16 v[32:47], v[156:159], v[72:79], v162\n\t" \
  "s_mov_b32 m0, s81\n\t" \
  "s_add_u32 s81, s81, s83\n\t" \
  "buffer_load_dwordx4 v163, s[72:75], s80 offen lds\n\t" \
  "s_add_u32 s80, s80, s82\n\t" \
  X2(82, 83) "s_waitcnt vmcnt(17)\n\t" \
  "v_mfma_f32_32x32x16_bf16 v[64:79], v[112:115], v[96:99], 0\n\t" \
  X2(84, 85) X2(86, 87) X2(88, 89) "v_smfmac_f32_32x32x32_bf16 v[48:63], v[152:155], v[80:87], v162\n\t" \
  X2(90, 91) X2(92, 93) X2(94, 95) X2(64, 65) "v_smfmac_f32_32x32x32_bf16 v[48:63], v[156:159], v[88:95], v162\n\t" \
  "s_mov_b64 exec, s[92:93]\n\t" \
  "buffer_load_dwordx4 v[152:155], v161, s[68:71], s90 offen offset:3072 sc1\n\t" \
  "buffer_load_dwordx4 v[156:159], v161, s[68:71], s90 offen offset:3584 sc1\n\t" \
  "s_mov_b64 exec, -1\n\t" \
  "buffer_load_dwordx4 v[196:199], v164, s[76:79], s86 offen\n\t" \
  "s_add_u32 s86, s86, 0x2000\n\t" \
  "global_load_dword v216, v[220:221], off\n\t" \
  X2(66, 67) "v_mfma_f32_32x32x16_bf16 v[80:95], v[112:115], v[100:103], 0\n\t" \
  X2(68, 69) X2(70, 71) X2(72, 73) "s_waitcnt vmcnt(18)\n\t" \
  "v_smfmac_f32_32x32x32_bf16 v[0:15], v[128:131], v[64:71], v162\n\t" \
  X2(74, 75) X2(76, 77) X2(78, 79) X2(80, 81) "v_smfmac_f32_32x32x32_bf16 v[0:15], v[132:135], v[72:79], v162\n\t" \
  "s_mov_b32 m0, s81\n\t" \
  "s_add_u32 s81, s81, s83\n\t" \
  "buffer_load_dwordx4 v163, s[72:75], s80 offen lds\n\t" \
  "s_add_u32 s80, s80, s82\n\t" \
  X2(82, 83) "v_mfma_f32_32x32x16_bf16 v[64:79], v[112:115], v[104:107], 0\n\t" \
  X2(84, 85) X2(86, 87) X2(88, 89) "v_smfmac_f32_32x32x32_bf16 v[16:31], v[128:131], v[80:87], v162\n\t" \
  X2(90, 91) X2(92, 93) X2(94, 95) X2(64, 65) "v_smfmac_f32_32x32x32_bf16 v[16:31], v[132:135], v[88:95], v162\n\t" \
  "global_load_dwordx2 v[212:213], v[218:219], off\n\t" \
  X2(66, 67) "v_mfma_f32_32x32x16_bf16 v[80:95], v[112:115], v[108:111], 0\n\t" \
  X2(68, 69) X2(70, 71) X2(72, 73) "v_smfmac_f32_32x32x32_bf16 v[32:47], v[128:131], v[64:71], v162\n\t" \
  X2(74, 75) X2(76, 77) X2(78, 79) X2(80, 81) "v_smfmac_f32_32x32x32_bf16 v[32:47], v[132:135], v[72:79], v162\n\t" \
  "s_mov_b32 m0, s81\n\t" \
  "s_add_u32 s81, s81, s83\n\t" \
  "buffer_load_dwordx4 v163, s[72:75], s80 offen lds\n\t" \
  "s_add_u32 s80, s80, s82\n\t" \
  X2(82, 83) "s_waitcnt vmcnt(19)\n\t" \
  "v_mfma_f32_32x32x16_bf16 v[64:79], v[116:119], v[96:99], 0\n\t" \
  X2(84, 85) X2(86, 87) X2(88, 89) "v_smfmac_f32_32x32x32_bf16 v[48:63], v[128:131], v[80:87], v162\n\t" \
  X2(90, 91) X2(92, 93) X2(94, 95) X2(64, 65) "v_smfmac_f32_32x32x32_bf16 v[48:63], v[132:135], v[88:95], v162\n\t" \
  "global_load_dwordx2 v[214:215], v[218:219], off offset:32\n\t" \
  X2(66, 67) "v_mfma_f32_32x32x16_bf16 v[80:95], v[116:119], v[100:103], 0\n\t" \
  X2(68, 69) X2(70, 71) X2(72, 73) "s_waitcnt vmcnt(17)\n\t" \
  "v_smfmac_f32_32x32x32_bf16 v[0:15], v[136:139], v[64:71], v162\n\t" \
  X2(74, 75) X2(76, 77) X2(78, 79) X2(80, 81) "v_smfmac_f32_32x32x32_bf16 v[0:15], v[140:143], v[72:79], v162\n\t" \
  "buffer_load_dwordx4 v[200:203], v163, s[72:75], s80 offen\n\t" \
  "s_add_u32 s80, s80, s82\n\t" \
  X2(82, 83) "v_mfma_f32_32x32x16_bf16 v[64:79], v[116:119], v[104:107], 0\n\t" \
  X2(84, 85) X2(86, 87) X2(88, 89) "v_smfmac_f32_32x32x32_bf16 v[16:31], v[136:139], v[80:87], v162\n\t" \
  "s_setprio 1\n\t" \
  X2(90, 91) X2(92, 93) X2(94, 95) X2(64, 65) "v_smfmac_f32_32x32x32_bf16 v[16:31], v[140:143], v[88:95], v162\n\t" \
  "global_load_dword v217, v[224:225], off\n\t" \
  X2(66, 67) "v_mfma_f32_32x32x16_bf16 v[80:95], v[116:119], v[108:111], 0\n\t" \
  X2(68, 69) X2(70, 71) X2(72, 73) "v_smfmac_f32_32x32x32_bf16 v[32:47], v[136:139], v[64:71], v162\n\t" \
  X2(74, 75) X2(76, 77) X2(78, 79) X2(80, 81) "v_smfmac_f32_32x32x32_bf16 v[32:47], v[140:143], v[72:79], v162\n\t" \
  "buffer_load_dwordx4 v[204:207], v163, s[72:75], s80 offen\n\t" \
  "s_add_u32 s80, s80, s82\n\t" \
  X2(82, 83) "s_waitcnt vmcnt(18)\n\t" \
  "v_mfma_f32_32x32x16_bf16 v[64:79], v[120:123], v[96:99], 0\n\t" \
  X2(84, 85) X2(86, 87) X2(88, 89) "v_smfmac_f32_32x32x32_bf16 v[48:63], v[136:139], v[80:87], v162\n\t" \
  X2(90, 91) X2(92, 93) X2(94, 95) X2(64, 65) "v_smfmac_f32_32x32x32_bf16 v[48:63], v[140:143], v[88:95], v162\n\t" \
  X2(66, 67) "v_mfma_f32_32x32x16_bf16 v[80:95], v[120:123], v[100:103], 0\n\t" \
  X2(68, 69) X2(70, 71) X2(72, 73) "s_waitcnt vmcnt(15)\n\t" \
  "v_smfmac_f32_32x32x32_bf16 v[0:15], v[144:147], v[64:71], v162\n\t" \
  X2(74, 75) X2(76, 77) X2(78, 79) X2(80, 81) "v_smfmac_f32_32x32x32_bf16 v[0:15], v[148:151], v[72:79], v162\n\t" \
  "buffer_load_dwordx4 v[208:211], v163, s[72:75], s80 offen\n\t" \
  "s_add_u32 s80, s80, s82\n\t" \
  X2(82, 83) "v_mfma_f32_32x32x16_bf16 v[64:79], v[120:123], v[104:107], 0\n\t" \
  X2(84, 85) X2(86, 87) X2(88, 89) "v_smfmac_f32_32x32x32_bf16 v[16:31], v[144:147], v[80:87], v162\n\t" \
  X2(90, 91) X2(92, 93) X2(94, 95) X2(64, 65) "v_smfmac_f32_32x32x32_bf16 v[16:31], v[148:151], v[88:95], v162\n\t" \
  X2(66, 67) "v_mfma_f32_32x32x16_bf16 v[80:95], v[120:123], v[108:111], 0\n\t" \
  X2(68, 69) X2(70, 71) X2(72, 73) "v_smfmac_f32_32x32x32_bf16 v[32:47], v[144:147], v[64:71], v162\n\t" \
  X2(74, 75) X2(76, 77) X2(78, 79) X2(80, 81) "v_smfmac_f32_32x32x32_bf16 v[32:47], v[148:151], v[72:79], v162\n\t" \
  X2(82, 83) "s_waitcnt vmcnt(13)\n\t" \
  "v_mfma_f32_32x32x16_bf16 v[64:79], v[124:127], v[96:99], 0\n\t" \
  X2(84, 85) X2(86, 87) X2(88, 89) "v_smfmac_f32_32x32x32_bf16 v[48:63], v[144:147], v[80:87], v162\n\t" \
  X2(90, 91) X2(92, 93) X2(94, 95) X2(64, 65) "v_smfmac_f32_32x32x32_bf16 v[48:63], v[148:151], v[88:95], v162\n\t" \
  X2(66, 67) "v_mfma_f32_32x32x16_bf16 v[80:95], v[124:127], v[100:103], 0\n\t" \
  X2(68, 69) X2(70, 71) X2(72, 73) "s_waitcnt vmcnt(10)\n\t" \
  "v_smfmac_f32_32x32x32_bf16 v[0:15], v[152:155], v[64:71], v162\n\t" \
  "s_setprio 0\n\t" \
  X2(74, 75) X2(76, 77) X2(78, 79) X2(80, 81) "v_smfmac_f32_32x32x32_bf16 v[0:15], v[156:159], v[72:79], v162\n\t" \
  X2(82, 83) "v_mfma_f32_32x32x16_bf16 v[64:79], v[124:127], v[104:107], 0\n\t" \
  X2(84, 85) X2(86, 87) X2(88, 89) "v_smfmac_f32_32x32x32_bf16 v[16:31], v[152:155], v[80:87], v162\n\t" \
  X2(90, 91) X2(92, 93) X2(94, 95) "ds_write_b128 v226, v[0:3]\n\t" \
  "ds_write_b128 v226, v[4:7] offset:1024\n\t" \
  "ds_write_b32 v229, v8 offset:2048\n\t" \
  X2(64, 65) "v_smfmac_f32_32x32x32_bf16 v[16:31], v[156:159], v[88:95], v162\n\t" \
  X2(66, 67) "v_mfma_f32_32x32x16_bf16 v[80:95], v[124:127], v[108:111], 0\n\t" \
  X2(68, 69) X2(70, 71) X2(72, 73) "v_smfmac_f32_32x32x32_bf16 v[32:47], v[152:155], v[64:71], v162\n\t" \
  X2(74, 75) X2(76, 77) X2(78, 79) "ds_write_b128 v227, v[16:19]\n\t" \
  "ds_write_b128 v227, v[20:23] offset:1024\n\t" \
  "ds_write_b32 v230, v24 offset:2048\n\t" \
  X2(80, 81) "v_smfmac_f32_32x32x32_bf16 v[32:47], v[156:159], v[72:79], v162\n\t" \
  X2(82, 83) X2(84, 85) X2(86, 87) X2(88, 89) "v_smfmac_f32_32x32x32_bf16 v[48:63], v[152:155], v[80:87], v162\n\t" \
  X2(90, 91) X2(92, 93) X2(94, 95) "ds_write_b128 v228, v[32:35]\n\t" \
  "ds_write_b128 v228, v[36:39] offset:1024\n\t" \
  "ds_write_b32 v231, v40 offset:2048\n\t" \
  "s_nop 1\n\t" \
  "v_smfmac_f32_32x32x32_bf16 v[48:63], v[156:159], v[88:95], v162\n\t" \
  "s_waitcnt vmcnt(0)\n\t" \
  "s_nop 12\n\t"
#define ATTN_LOOP_CLOBBERS "v64", "v65", "v66", "v67", "v68", "v69", "v70", "v71", "v72", "v73", "v74", "v75", "v76", "v77", "v78", "v79", "v80", "v81", "v82", "v83", "v84", "v85", "v86", "v87", "v88", "v89", "v90", "v91", "v92", "v93", "v94", "v95", "v112", "v113", "v114", "v115", "v116", "v117", "v118", "v119", "v120", "v121", "v122", "v123", "v124", "v125", "v126", "v127", "v128", "v129", "v130", "v131", "v132", "v133", "v134", "v135", "v136", "v137", "v138", "v139", "v140", "v141", "v142", "v143", "v144", "v145", "v146", "v147", "v148", "v149", "v150", "v151", "v152", "v153", "v154", "v155", "v156", "v157", "v158", "v159", "v217", "v226", "v227", "v228", "v229", "v230", "v231", "v0", "v1", "v2", "v3", "v4", "v5", "v6", "v7", "v8", "v9", "v10", "v11", "v12", "v13", "v14", "v15", "v16", "v17", "v18", "v19", "v20", "v21", "v22", "v23", "v24", "v25", "v26", "v27", "v28", "v29", "v30", "v31", "v32", "v33", "v34", "v35", "v36", "v37", "v38", "v39", "v40", "v41", "v42", "v43", "v44", "v45", "v46", "v47", "s88", "s89", "s90", "s92", "s93"

constexpr int SLOT = 24576;
constexpr int L_WQ = 122880;
constexpr int L_P1T = 155648;
constexpr int L_W1 = 0;
constexpr int L_RED = 102400;
constexpr int L_Q = 94208;
constexpr int L_WO = 93184;
constexpr int L_EX = 131072;
constexpr int L_H = 147456;
constexpr int L_TAB = 158720;
constexpr int L_CNT = 160768;
constexpr int L_DUM = 162816;

DEVI unsigned split_hl(float v) {
  const unsigned hi = pk2(v, v) & 0xffff0000u;
  return (hi >> 16) | (pk2(0.f, v - u2f(hi)) & 0xffff0000u);
}
DEVI u64 mk_tag(v2u ep) {
  const u64 t = (u64)(unsigned)__builtin_amdgcn_readfirstlane(ep.x) | ((u64)(unsigned)__builtin_amdgcn_readfirstlane(ep.y) << 32);
  return (t ^ (t >> 31)) * 0x9E3779B97F4A7C15ull + 0x632BE59BD9B4E019ull;
}

template <bool ISV>
DEVI void p1_compute(LAS char* L, int tb, int l, rsrc_t rws, int sgo, unsigned tag, int kvo) {
  const int r = l & 31, h = l >> 5, row = tb * 32 + r;
  const int c0 = (2 * tb + h) ^ ((row >> 1) & 7);
  LAS char* xa = L + (row >> 3) * 1024 + (row & 7) * 128;
  const int q = (l & 15) >> 2;
  const int wc = (2 * ((l >> 4) & 1) + ((l >> 1) & 1)) ^ ((q & 2) << 1);
  LAS char* wa0 = L + 16384 + (8 * h + q) * 128 + wc * 16 + (l & 1) * 8;
  LAS char* wa1 = L + 16384 + (8 * h + q) * 128 + (wc ^ 4) * 16 + (l & 1) * 8;
  LAS unsigned* csl = (LAS unsigned*)(L + L_P1T);
  LAS float* prv = (LAS float*)(L + L_P1T + 4096) + tb * 64;
  LAS unsigned* cnt = (LAS unsigned*)(L + L_P1T + 2048);
  const int slot = (r & 19) | ((r & 4) << 1) | ((r & 8) >> 1);
  int vo = (ISV ? (r >> 4) * 65536 + h * 256 + (r & 15) * 16 : (h * 32 + slot) * 16) + kvo;
  asm volatile("" : "+v"(vo));
  v4u xf[2][4], wf[2][4][2], sg, cv[2];
  f32x16 acc[2] = {{0}, {0}}, rv;
  f32x4 cc[2] = {{0}, {0}};
  unsigned mh;
#pragma unroll
  for (int c = 0; c < 9; ++c) {
    const int b = (c - 1) & 1;
    if (c < 8) {
      BAR();
      SCHED();
      if (c == 4) sg = ld128c(rws, r * 16, sgo);
    }
    if (c > 0) {
      cc[0] = mfma4(ONES2, ONES2, wf[b][0][0].x, wf[b][0][0].y, cc[0]); cc[1] = mfma4(ONES2, ONES2, wf[b][0][1].x, wf[b][0][1].y, cc[1]);
      cc[0] = mfma4(ONES2, ONES2, wf[b][0][0].z, wf[b][0][0].w, cc[0]); cc[1] = mfma4(ONES2, ONES2, wf[b][0][1].z, wf[b][0][1].w, cc[1]);
      SCHED();
    }
#pragma unroll
    for (int p = 0; p < 4; ++p) {
      if (c > 0) acc[0] = ISV ? mfma32(xf[b][p], wf[b][p][0], acc[0]) : mfma32(wf[b][p][0], xf[b][p], acc[0]);
      SCHED();
      if (c < 8) {
        xf[c & 1][p] = *(LAS v4u*)(xa + (c % 5) * SLOT + 16 * ((2 * p) ^ c0));
        wf[c & 1][p][0] = trfrag(wa0 + (c % 5) * SLOT + ((p ^ tb) * 2048), 512);
      }
      if (c == 8 && p == 0) { csl[l * 4 + tb] = split_hl(swap32_add(cc[0][0], cc[1][0])); lds_post(cnt, l); }
      if (c == 8 && p == 2) { lds_wait(cnt, 4); cv[0] = *(LAS v4u*)(csl + r * 4); cv[1] = *(LAS v4u*)(csl + (32 + r) * 4); }
      SCHED();
      if (c > 0) acc[1] = ISV ? mfma32(xf[b][p], wf[b][p][1], acc[1]) : mfma32(wf[b][p][1], xf[b][p], acc[1]);
      SCHED();
      if (c < 8) wf[c & 1][p][1] = trfrag(wa1 + (c % 5) * SLOT + ((p ^ tb) * 2048), 512);
      SCHED();
    }
    if (c == 6) {
      _Pragma("unroll 1") for (int sp = 0; sp < (1 << 18); ++sp) {
        if (wave_all(sg.y == tag && sg.w == tag)) break;
        MEMBAR();
        sg = ld128c(rws, r * 16, sgo);
      }
      mh = __builtin_amdgcn_perm(sg.z, sg.z, h ? 0x03020302u : 0x01000100u);
      if (ISV) {
        prv[l] = u2f(sg.x);
#pragma unroll
        for (int gg = 0; gg < 4; ++gg) {
          const f32x4 a = *(LAS f32x4*)(prv + 8 * gg + 4 * h);
#pragma unroll
          for (int e = 0; e < 4; ++e) rv[4 * gg + e] = a[e];
        }
      }
      SCHED();
    }
  }
  const v4u mv = {mh, mh, mh, mh};
#pragma unroll
  for (int cb = 0; cb < 2; ++cb) acc[cb] = ISV ? mfma32(mv, cv[cb], acc[cb]) : mfma32(cv[cb], mv, acc[cb]);
#pragma unroll
  for (int cb = 0; cb < 2; ++cb) {
    const f32x16 v = ISV ? acc[cb] * rv : acc[cb] * u2f(sg.x);
    st128c(v4u{pk2(v[0], v[1]), pk2(v[2], v[3]), pk2(v[4], v[5]), pk2(v[6], v[7])}, rws, vo, cb * 131072);
    st128c(v4u{pk2(v[8], v[9]), pk2(v[10], v[11]), pk2(v[12], v[13]), pk2(v[14], v[15])}, rws, vo, cb * 131072 + (ISV ? 512 : 65536));
  }
}

__global__ __launch_bounds__(512, 2) NOPK void k_fused(const char* __restrict__ x, const char* __restrict__ wqkv, const char* __restrict__ wo,
                                                  const char* __restrict__ w1, const char* __restrict__ b1, const char* __restrict__ w2,
                                                  const char* __restrict__ b2, char* __restrict__ ws, char* __restrict__ out) {
  extern __shared__ __attribute__((aligned(16))) char smem_[];
  LAS char* L = (LAS char*)smem_;
  const unsigned L0 = (unsigned)(size_t)L;
  const int l = threadIdx.x & 63, w = __builtin_amdgcn_readfirstlane(threadIdx.x >> 6);
  const int xcd = blockIdx.x & 7, idx = blockIdx.x >> 3;
  const int ti = (xcd >> 1) * 4 + (idx >> 3), hj = (xcd & 1) * 8 + (idx & 7);
  const int i2 = xcd * 2 + (idx >> 4), j2 = idx & 15;
  const int th = j2 >> 3, cb2 = j2 & 7;
  const rsrc_t rx = mk_rsrc(x, 2048 * 1024), rws = mk_rsrc(ws, WS_BYTES);
  unsigned cpf = 0;
  if (w == 0) {
    u64 pc; unsigned len;
    asm volatile("s_getpc_b64 %0\n.Lfused_begin:\n\ts_mov_b32 %1, .Lfused_end-.Lfused_begin" : "=s"(pc), "=s"(len));
    cpf = __builtin_amdgcn_raw_buffer_load_b32(mk_rsrc((const void*)(pc & ~127ull), len), (l + ((blockIdx.x >> 3) & 7) * 64) * 128, 0, 0);
  }
  u64 tag;
  const int tk = l & 15, g = l >> 4;
  const int tg = i2 * 8 + w;
  const int tg3 = i2 * 8 + (w & 2) * 2 + th * 2 + (w & 1);
  const int w2v = (l >> 3) * 1024 + (((l & 7) ^ (((l >> 4) & 3) << 1)) * 16);
  const int w2u = (l >> 3) * 1024 + (((l & 7) ^ (((l >> 4) & 1) << 2)) * 16);
  v2u x0, x1;
  unsigned b1v;
  const unsigned vc = ((l & 31) == 16 || (l & 31) == 20) ? ONES2 : 0u;
  v16u sA; v8u sB, vP; const char *xp, *bp, *t1p, *t2p;
  auto attn_setup = [&]() __attribute__((always_inline)) {
    const int head = hj * 2 + (w >> 2), kq = w & 3;
    const int vv = (l >> 5) * 256 + (l & 15) * 16;
    const v4u sk = mk_srd(ws + KF_OFF + (head * 64 + kq * 16) * 1024, 16 * 1024);
    const v4u sv = mk_srd(ws + VF_OFF + (head * 64 + kq * 16) * 1024, 16 * 1024);
    const v4u sd = w < 7 ? mk_srd(w1, 512 * 3584) : mk_srd(w2, (idx * 7 + 7) * 8192), so = mk_srd(wo, 512 * 1024);
    const int c = w * 64 + l;
    const int dof = w < 7 ? (c / 14) * 3584 + (c % 14) * 16 : l * 128;
    const unsigned sdo = w < 7 ? j2 * 224 : (idx * 7 + 15) * 8192;
    const unsigned lm = w < 7 ? L0 + L_W1 + w * 1024 : L0 + L_DUM;
    const unsigned lstep = w < 7 ? 7168 : 0, dstep = w < 7 ? 32 * 3584 : -8192;
    sA = v16u {sk.x, sk.y, sk.z, sk.w, sv.x, sv.y, sv.z, sv.w, sd.x, sd.y, sd.z, sd.w, so.x, so.y, so.z, so.w};
    sB = v8u {sdo, lm, dstep, lstep, L0 + L_RED + ((w ^ 3) * 3) * 2304, L0 + L_RED + ((w ^ 2) * 3 + 1) * 2304, (unsigned)(cb2 * 128 + w * 65536), L0 + L_RED + ((w ^ 1) * 3 + 2) * 2304};
    vP = v8u {(unsigned)l * 16, (unsigned)vv, 0xddddu, (unsigned)dof, (unsigned)w2v,
              L0 + L_Q + w * 1024 + l * 16, vc, vc};
    xp = x + (tg3 * 16 + tk) * 1024 + (cb2 * 64 + (w >> 2) * 32 + 4 * g) * 2;
    bp = w == 7 ? b1 + j2 * 224 + (l < 56 ? l : 0) * 4 : b2 + cb2 * 128;
    t1p = wo + ((idx * 8 + w) * 16 + (l & 15)) * 128;
    {
      u64 pc; unsigned o1, o2;
      asm volatile("s_getpc_b64 %0\n.Lfused_here:\n\ts_mov_b32 %1, .Lfused_tail-.Lfused_here\n\ts_mov_b32 %2, .Lfused_end-.Lfused_here" : "=s"(pc), "=s"(o1), "=s"(o2));
      t2p = (const char*)pc + __builtin_elementwise_min(o1 + (unsigned)(((idx & 3) * 8 + w) * 8 + (l & 7)) * 128u, o2);
      if (l >= 32) t2p = b2 + cb2 * 128;
    }
    asm volatile("" : "+{v[160:167]}"(vP), "+{s[64:79]}"(sA), "+{s[80:87]}"(sB), "+{v[218:219]}"(xp), "+{v[220:221]}"(bp), "+{v[222:223]}"(t1p), "+{v[224:225]}"(t2p));
  };
  {
    const int tb = w & 3, r = l & 31, h = l >> 5;
    const int rho = ((idx & 7) + 2 * (idx >> 3)) & 7;
    const int sgo = SG_OFF + (ti * 128 + tb * 32) * 16;
    LAS unsigned* csq = (LAS unsigned*)(L + L_P1T) + 256;
    LAS unsigned* cnt = (LAS unsigned*)(L + L_P1T + 2048);
    const v4u sx = mk_srd(x, 2048 * 1024), sw = mk_srd(wqkv, 512 * 3072);
    const int xv0 = (l >> 3) * 1024 + 16 * ((l & 7) ^ (l >> 4));
    const int xv1 = (l >> 3) * 1024 + 16 * ((l & 7) ^ (4 + (l >> 4)));
    const int xs = (ti * 128 + tb * 32) * 1024;
    const int wv = (l >> 3) * 3072 + 16 * ((l & 7) ^ (((l >> 4) & 1) << 2));
    const int wsb = 1024 + hj * 128 + tb * 16 * 3072;
    const int qv = (l >> 2) * 3072 + (l & 3) * 16;
#define P1_X(c) _Pragma("unroll") for (int i_ = 0; i_ < 4; ++i_) dma16(sx, L0 + ((c) % 5) * SLOT + (4 * tb + i_) * 1024, (i_ & 1) ? xv1 : xv0, xs + i_ * 8192 + (((c) + rho) & 7) * 128)
#define P1_W(c) _Pragma("unroll") for (int i_ = 0; i_ < 2; ++i_) dma16(sw, L0 + ((c) % 5) * SLOT + 16384 + (2 * tb + i_) * 1024, wv, wsb + ((((c) + rho) & 7) * 64 + i_ * 8) * 3072)
    const int srow = ti * 128 + hj * 8 + 2 * w;
    v4u xr0, xr1; v2u ep; unsigned wpf;
    if (w < 4) {
      xr0 = ld128(rx, l * 16, srow * 1024); xr1 = ld128(rx, l * 16, srow * 1024 + 1024);
      ep = ld64c(rws, 0, EP_OFF);
      wpf = *(const GAS unsigned*)(wqkv + (idx * 16 + w * 4 + ((l >> 2) & 3)) * 3072 + (xcd & 1) * 512 + (l & 3) * 128);
    } else { P1_X(0); P1_W(0); P1_W(1); }
    SCHED();
    attn_setup();
    SCHED();
    if (w < 4) {
      if (w == 0) cnt[l & 3] = 0u;
      tag = mk_tag(ep);
      if (w == 0) *(LAS v2u*)(cnt + 4) = ep;
      {
        float a1 = 0.f, a2 = 0.f, b1_ = 0.f, b2_ = 0.f;
#pragma unroll
        for (int e = 0; e < 4; ++e) { a1 = dot2(xr0[e], ONES2, a1); a2 = dot2(xr0[e], xr0[e], a2); b1_ = dot2(xr1[e], ONES2, b1_); b2_ = dot2(xr1[e], xr1[e], b2_); }
        const float va = swap32_add(a1, a2), vb = swap32_add(b1_, b2_);
        auto rr = __builtin_amdgcn_permlane16_swap(f2u(va), f2u(vb), false, false);
        float t = u2f(rr[0]) + u2f(rr[1]);
        t = dpp_add<0x128>(t); t = dpp_add<0x124>(t); t = dpp_add<0x122>(t); t = dpp_add<0x121>(t);
        auto r2 = __builtin_amdgcn_permlane32_swap(f2u(t), f2u(t), false, false);
        const float mu = u2f(r2[0]) * (1.f / 512.f);
        const float rs = __builtin_amdgcn_rsqf(fmaxf(u2f(r2[1]) * (1.f / 512.f) - mu * mu, 0.f) + 1e-5f);
        st128c(v4u{f2u(rs), (unsigned)tag, split_hl(-mu), (unsigned)tag}, rws, (l & 47) ? 0x40000000 : SG_OFF + (srow + (l >> 4)) * 16, 0);
      }
      WAITL(0);
      const int kvo = KF_OFF + (hj * 4 * 64 + ti * 4 + tb) * 1024;
      if (hj >= 8) p1_compute<true>(L, tb, l, rws, sgo, (unsigned)tag, kvo);
      else p1_compute<false>(L, tb, l, rws, sgo, (unsigned)tag, kvo);
      const Flag fl = flag_prep(0, hj, ti, tag, l);
      WAITV(0);
      if (lds_inc(cnt + 1, l) == 3) st128c(fl.d, rws, fl.o, 0);
      asm volatile("" ::"v"(wpf), "v"(cpf));
    } else {
      P1_X(1);
      const int row = tb * 32 + r;
      const int c0 = (2 * tb + h) ^ ((row >> 1) & 7);
      LAS char* xa = L + (row >> 3) * 1024 + (row & 7) * 128;
      v4u xq[32];
      v4u sg;
#pragma unroll
      for (int c = 0; c < 8; ++c) {
        if (c < 7) WAITV(6); else WAITV(3);
        BAR();
        SCHED();
#pragma unroll
        for (int p = 0; p < 4; ++p) xq[c * 4 + p] = *(LAS v4u*)(xa + (c % 5) * SLOT + 16 * ((2 * p) ^ c0));
        if (c < 6) { P1_X(c + 2); P1_W(c + 2); WAITL(0); }
        else {
#pragma unroll
          for (int i = (c - 6) * 3; i < (c == 6 ? 3 : 8); ++i) dma16(sw, L0 + L_WQ + (tb * 8 + i) * 1024, qv, hj * 64 + ((((tb * 2 + (i >> 2) + rho) & 7) * 4 + (i & 3)) * 16) * 3072);
        }
        SCHED();
      }
      sg = ld128c(rws, r * 16, sgo);
      tag = mk_tag(*(LAS v2u*)(cnt + 4));
      _Pragma("unroll 1") for (int sp = 0; sp < (1 << 18); ++sp) {
        if (wave_all(sg.y == (unsigned)tag && sg.w == (unsigned)tag)) break;
        MEMBAR();
        sg = ld128c(rws, r * 16, sgo);
      }
      WAITV(0);
      lds_post(cnt + 2, l);
      lds_wait(cnt + 2, 4);
      f32x16 aq = {0};
      {
        LAS char* wa = L + L_WQ + (8 * h + ((l & 15) >> 2)) * 64 + ((l >> 4) & 1) * 32 + (l & 3) * 8;
        v4u wq[33];
        f32x4 cq = {0}, cq2 = {0};
        LAS char* wp[4];
#pragma unroll
        for (int p = 0; p < 4; ++p) wp[p] = opq(wa + (p ^ tb) * 1024);
#define QORD(s) ((s) < 8 ? (s) * 4 : (((s) - 8) / 3) * 4 + ((s) - 8) % 3 + 1)
#pragma unroll
        for (int s = 0; s < 36; ++s) {
          if (s >= 4) aq = mfma32(wq[QORD(s - 4)], xq[QORD(s - 4)], aq);
          if (s == 12) {
#pragma unroll
            for (int u = 0; u < 32; u += 4) { cq = mfma4(ONES2, ONES2, wq[u].x, wq[u].y, cq); cq2 = mfma4(ONES2, ONES2, wq[u].z, wq[u].w, cq2); }
          }
          if (s == 16) { const float cs = cq[0] + cq2[0]; csq[r * 4 + tb] = split_hl(swap32_add(cs, cs)); lds_post(cnt + 3, l); }
          SCHED();
          if (s < 32) wq[QORD(s)] = trfrag(wp[QORD(s) & 3] + (QORD(s) >> 2) * 4096, 256);
          if (s == 32) { lds_wait(cnt + 3, 4); wq[32] = *(LAS v4u*)(csq + r * 4); }
          SCHED();
        }
        const unsigned mh = __builtin_amdgcn_perm(sg.z, sg.z, h ? 0x03020302u : 0x01000100u);
        aq = mfma32(wq[32], v4u{mh, mh, mh, mh}, aq);
      }
      aq *= u2f(sg.x) * 0.36067376022224085f;
      *(LAS v4u*)(L + L_Q + tb * 1024 + l * 16) = v4u{pk2(aq[0], aq[1]), pk2(aq[2], aq[3]), pk2(aq[4], aq[5]), pk2(aq[6], aq[7])};
      *(LAS v4u*)(L + L_Q + (4 + tb) * 1024 + l * 16) = v4u{pk2(aq[8], aq[9]), pk2(aq[10], aq[11]), pk2(aq[12], aq[13]), pk2(aq[14], aq[15])};
    }
  }
  v16u worA, worB; v4u w1r[3];
  {
    const int head = hj * 2 + (w >> 2), kq = w & 3;
    const int r = l & 31, h = l >> 5;
    const int vv = h * 256 + (r & 15) * 16;
    const int sidx = 0xdddd;
    if (w == 0) { GAS u64* fp = pin(flag_ptr(ws, 0, (hj >> 1) + ((l & 16) >> 1), l)); __builtin_amdgcn_s_sleep(20); flag_wait(fp, tag); }
    LBAR();
    f32x16 o0;
    v4u q[4];
    asm volatile(ATTN_LOOP_ASM
                 : "={v[48:63]}"(o0), "={v[96:99]}"(q[0]), "={v[100:103]}"(q[1]),
                   "={v[104:107]}"(q[2]), "={v[108:111]}"(q[3]), "={v[168:183]}"(worA), "={v[184:199]}"(worB), "={v[200:203]}"(w1r[0]),
                   "={v[204:207]}"(w1r[1]), "={v[208:211]}"(w1r[2]), "={v[212:213]}"(x0), "={v[214:215]}"(x1), "={v216}"(b1v), "+{s[80:87]}"(sB)
                 : "{v[160:167]}"(vP), "{s[64:79]}"(sA), "{v[218:219]}"(xp), "{v[220:221]}"(bp), "{v[222:223]}"(t1p), "{v[224:225]}"(t2p)
                 : "memory", "scc", ATTN_LOOP_CLOBBERS);
    asm volatile(".Lfused_tail:");
    f32x4 a0 = {o0[0], o0[1], o0[2], o0[3]}, a1 = {o0[4], o0[5], o0[6], o0[7]};
    float ls = o0[8];
    int oo_ = (r >> 4) * 16384 + ((r & 15) + 16 * (2 * (w >> 2) + h)) * 16 + OF_OFF + ((ti * 8 + kq * 2) * 16 + hj) * 1024;
    LAS char* rp = opq(L + L_RED + w * 6912 + l * 16);
    LAS char* rl = opq(L + L_RED + w * 6912 + 2048 + l * 4);
    asm volatile("" : "+v"(oo_));
    LBAR();
#pragma unroll
    for (int s = 0; s < 3; ++s) {
      a0 += *(LAS f32x4*)(rp + s * 2304);
      a1 += *(LAS f32x4*)(rp + s * 2304 + 1024);
      ls += *(LAS float*)(rl + s * 2304);
    }
#define O_STORE() do { const float inv = __builtin_amdgcn_rcpf(ls); \
      st128c(v4u{pk2(a0[0] * inv, a0[1] * inv), pk2(a0[2] * inv, a0[3] * inv), pk2(a1[0] * inv, a1[1] * inv), pk2(a1[2] * inv, a1[3] * inv)}, rws, oo_, 0); } while (0)
    O_STORE();
    SCHED();
    const float asum = ((fabsf(a0[0]) + fabsf(a0[1])) + (fabsf(a0[2]) + fabsf(a0[3]))) + ((fabsf(a1[0]) + fabsf(a1[1])) + (fabsf(a1[2]) + fabsf(a1[3])));
    const bool okl = (ls >= 0x1p-60f) && (ls <= 0x1p120f) && (asum <= 0x1p126f);
    if (__builtin_expect(!wave_all(okl), 0)) {
      const f32x16 z = {0};
      const v4u vcc = {vc, vc, vc, vc};
      const v4u qq = q[3];
      const int kb0 = KF_OFF + head * 65536, vb0 = VF_OFF + head * 65536;
      float m = -3.0e38f;
      for (int t = 0; t < 64; ++t) {
        v4u kk = ld128c(rws, l * 16, kb0 + t * 1024);
        f32x16 s = mfma32(kk, qq, z);
#pragma unroll
        for (int e = 0; e < 16; ++e) m = fmaxf(m, s[e]);
      }
      { auto mm = __builtin_amdgcn_permlane32_swap(f2u(m), f2u(m), false, false); m = fmaxf(u2f(mm[0]), u2f(mm[1])); }
      f32x16 oo = {0};
      for (int t = 0; t < 64; ++t) {
        v4u kk = ld128c(rws, l * 16, kb0 + t * 1024);
        v4u va = ld128c(rws, vv, vb0 + t * 1024);
        v4u vbb = ld128c(rws, vv + 512, vb0 + t * 1024);
        f32x16 s = mfma32(kk, qq, z);
#pragma unroll
        for (int e = 0; e < 16; ++e) s[e] = __builtin_amdgcn_exp2f(s[e] - m);
        oo = smf(r < 16 ? va : vcc, lo8(s), oo, sidx);
        oo = smf(r < 16 ? vbb : vcc, hi8(s), oo, sidx);
      }
      a0 = f32x4{oo[0], oo[1], oo[2], oo[3]}; a1 = f32x4{oo[4], oo[5], oo[6], oo[7]}; ls = oo[8];
      WAITV(0);
      O_STORE();
    }
    const Flag fl = flag_prep(1, ti, hj, tag, l);
    WAITV(0);
    LBAR();
    if (w == 0) st128c(fl.d, rws, fl.o, 0);
  }
  const v4u s2w = mk_srd(w2, 1792 * 1024);
  const rsrc_t rw2 = mk_rsrc(w2, 1792 * 1024);
  const int team = w >> 2, kq = w & 3;
  LAS char* w1a = opq(L + L_W1 + (4 * g + (tk >> 2)) * 224 + (l & 3) * 8);
  LAS char* w1b = opq(w1a + 8 * 7168);
#define W1_FRAG(s, nb) trfrag(((s) < 8 ? w1a : w1b) + (nb) * 32 + ((s) & 7) * 7168, 3584)
  LAS unsigned* cn = (LAS unsigned*)(L + L_CNT);
  v4u pre[16][2], bf1[2][5];
  {
#pragma unroll
    for (int t = 0; t < 4; ++t) {
      *(LAS v4u*)(L + L_WO + (w * 8 + t) * 1024 + l * 16) = v4u{worA[4 * t], worA[4 * t + 1], worA[4 * t + 2], worA[4 * t + 3]};
      *(LAS v4u*)(L + L_WO + (w * 8 + 4 + t) * 1024 + l * 16) = v4u{worB[4 * t], worB[4 * t + 1], worB[4 * t + 2], worB[4 * t + 3]};
    }
    if (w == 7) {
      {
        const int l = threadIdx.x < 504 ? (threadIdx.x & 63) : 0;
        *(LAS v2u*)(L + L_TAB + l * 32 + 8) = v2u{__builtin_amdgcn_perm(b1v, b1v, 0x01000100u), b1v & 0xffffu};
        *(LAS v2u*)(L + L_TAB + l * 32 + 24) = v2u{__builtin_amdgcn_perm(b1v, b1v, 0x03020302u), b1v >> 16};
      }
      cn[l & 15] = 0u;
    }
    LBAR();
    GAS u64* fp = pin(flag_ptr(ws, 1, i2, l));
    const int ob0 = pin(OF_OFF + (tg3 * 16 + team * 8) * 1024), ob1 = pin(ob0 + 4096);
    v4u wf[8][4];
    {
      const int rq = 4 * (g & 1) + (tk >> 2);
      LAS char* wa = L + L_WO + (team * 256 + 16 * (g >> 1) + rq) * 128 + (l & 1) * 8;
      const int sw = ((rq >> 1) & 3) << 1, c1 = (l >> 1) & 1;
#pragma unroll
      for (int nb = 0; nb < 4; ++nb) {
        LAS char* wn = opq(wa + (((nb * 2 + c1) ^ sw ^ (team * 4)) * 16));
#pragma unroll
        for (int s = 0; s < 8; ++s) wf[s][nb] = trfrag(wn + s * 4096, 1024);
      }
    }
    if (w == 0) flag_wait(fp, tag);
    LBAR();
    v4u a[8];
#pragma unroll
    for (int s = 0; s < 8; ++s) { a[s] = ld128c(rws, l * 16 + (s & 3) * 1024, s < 4 ? ob0 : ob1); SCHED(); }
    if (w < 7) {
#pragma unroll
      for (int t = 0; t < 3; ++t) *(LAS v4u*)(L + L_W1 + (13 + t) * 7168 + w * 1024 + l * 16) = w1r[t];
    }
    SCHED();
    f32x4 c[4] = {{bflo(x0.x), bfhi(x0.x), bflo(x0.y), bfhi(x0.y)}, {bflo(x1.x), bfhi(x1.x), bflo(x1.y), bfhi(x1.y)}, {0}, {0}};
    LAS char* xs = opq(L + L_EX + w * 2048 + l * 16);
    LAS char* xr = opq(L + L_EX + (w ^ 4) * 2048 + l * 16);
    int ho = l * 16 + HF_OFF + (tg3 * 16 + cb2 * 2 + team) * 1024;
    asm volatile("" : "+v"(ho), "+v"(c[0]), "+v"(c[1]), "+v"(xs), "+v"(xr));
    SCHED();
#pragma unroll
    for (int s = 0; s < 8; ++s)
#pragma unroll
      for (int nb = 3; nb >= 0; --nb) c[nb] = mfma16(wf[s][nb], a[s], c[nb]);
    *(LAS f32x4*)(xs + 1024) = c[3];
    *(LAS f32x4*)xs = c[2];
    LBAR();
    const f32x4 c0 = c[0] + *(LAS f32x4*)xr, c1 = c[1] + *(LAS f32x4*)(xr + 1024);
    const v4u hf = {pk2(c0[0], c0[1]), pk2(c0[2], c0[3]), pk2(c1[0], c1[1]), pk2(c1[2], c1[3])};
    st128c(hf, rws, ho, 0);
    float s1 = 0.f, s2 = 0.f;
#pragma unroll
    for (int e = 0; e < 4; ++e) { s1 = dot2(hf[e], ONES2, s1); s2 = dot2(hf[e], hf[e], s2); }
    const float s12 = swap16_add(swap32_add(s1, s2));
    __builtin_amdgcn_raw_buffer_store_b32(f2u(s12), rws, tk * 8 + (l >> 5) * 4 + ((l & 16) << 26), ST_OFF + (tg3 * 16 + cb2 * 2 + team) * 128, SC1);
    SCHED();
    *(LAS v2u*)(L + L_H + (kq * 16 + tk) * 144 + team * 64 + g * 8) = v2u{hf.x, hf.y};
    *(LAS v2u*)(L + L_H + (kq * 16 + tk) * 144 + team * 64 + 32 + g * 8) = v2u{hf.z, hf.w};
#pragma unroll
    for (int s = 0; s < 12; ++s) { pre[s][0] = W1_FRAG(s, 5); pre[s][1] = W1_FRAG(s, 6); }
    const Flag fl = flag_prep(2, i2, j2, tag, l);
    WAITV(0);
    BAR();
    if (w == 0) st128c(fl.d, rws, fl.o, 0);
  }
  v4u w2r[10];
  v4u bb2;
  const int orow = threadIdx.x >> 3, oc = threadIdx.x & 7;
  {
    LAS char* tabp = opq(L + L_TAB + tk * 16);
    v4u (&bf)[2][5] = bf1;
#pragma unroll
    for (int s = 12; s < 16; ++s) { pre[s][0] = W1_FRAG(s, 5); pre[s][1] = W1_FRAG(s, 6); }
#pragma unroll
    for (int nb = 0; nb < 5; ++nb) bf[0][nb] = W1_FRAG(0, nb);
    const int hb0 = pin(HF_OFF + tg * 16384), hb1 = pin(hb0 + 4096);
    if (w == 0) flag_wait(flag_ptr(ws, 2, i2, l), tag);
    else {
      const v4u ones = {ONES2, ONES2, ONES2, ONES2};
      f32x4 cs = {0};
      v4u t[16];
      LAS char* ca = opq(w1a + (w - 1) * 32);
      LAS char* cb = opq(w1b + (w - 1) * 32);
#pragma unroll
      for (int s = 0; s < 16; ++s) t[s] = trfrag((s < 8 ? ca : cb) + (s & 7) * 7168, 3584);
      SCHED();
#pragma unroll
      for (int s = 0; s < 16; ++s) {
        cs = mfma16(t[s], ones, cs);
        if (s % 3 == 0 && s < 15) {
          const int m = __builtin_elementwise_min(w - 1 + (s / 3) * 7, 31);
          dma16(s2w, L0 + 7 * 16384 + m * 1024, w2u, cb2 * 128 + (((m >> 2) & 3) * 448 + (m >> 4) * 32 + (m & 3) * 8) * 1024);
        }
      }
      const unsigned hl = split_hl((tk & 2) ? ((tk & 1) ? cs[3] : cs[2]) : ((tk & 1) ? cs[1] : cs[0]));
      *(LAS v2u*)(L + L_TAB + ((w - 1) * 16 + 4 * g + (tk & 3)) * 16) = v2u{hl, hl};
    }
    LBAR();
    if (team == 0) __builtin_amdgcn_s_setprio(1); else __builtin_amdgcn_s_sleep(16);
    constexpr int WIN = 6;
#define H_LOAD(s) ld128c(rws, l * 16 + ((s) & 3) * 1024, (s) < 4 ? hb0 : (s) < 8 ? hb1 : hb0 + ((s) & 12) * 1024)
    v4u a[16];
    v2u sp[4];
#pragma unroll
    for (int s = 0; s < WIN; ++s) { a[s] = H_LOAD(s); SCHED(); }
    f32x4 c[7];
#pragma unroll
    for (int nb = 0; nb < 7; ++nb) c[nb] = f32x4{0};
    float sig, k0, k1, s1, s2; v4u mv;
#pragma unroll
    for (int s = 0; s < 16; ++s) {
      if (s < 5) { a[2 * s + WIN] = H_LOAD(2 * s + WIN); a[2 * s + WIN + 1] = H_LOAD(2 * s + WIN + 1); }
      if (s == 5) {
#pragma unroll
        for (int e = 0; e < 4; ++e) sp[e] = ld64c(rws, (4 * g + e) * 128 + tk * 8, ST_OFF + tg * 2048);
        bb2 = *(const GAS v4u*)(b2 + (cb2 * 64 + oc * 8) * 2);
      }
      if (s >= 11) {
#pragma unroll
        for (int u = 0; u < 2; ++u) w2r[2 * (s - 11) + u] = ld128(rw2, w2u, cb2 * 128 + (kq * 448 + (s - 2) * 32 + (team * 2 + u) * 8) * 1024);
      }
      if (s == 9) { s1 = (u2f(sp[0].x) + u2f(sp[1].x)) + (u2f(sp[2].x) + u2f(sp[3].x)); s2 = (u2f(sp[0].y) + u2f(sp[1].y)) + (u2f(sp[2].y) + u2f(sp[3].y)); s1 = swap32_add(s1, s2); }
      if (s == 10) { s1 = swap16_add(s1) * (1.f / 512.f); auto r2 = __builtin_amdgcn_permlane32_swap(f2u(s1), f2u(s1), false, false); s1 = u2f(r2[0]); s2 = u2f(r2[1]); }
      if (s == 11) {
        const float var = fmaxf(fmaf(-s1, s1, s2), 0.f);
        sig = s2 = __builtin_amdgcn_sqrtf(fmaf(1e-5f, var + 1e-5f, var));
      }
      if (s == 12) { const unsigned mhl = split_hl(g ? 0.f : -s1); mv.x = __builtin_amdgcn_perm(mhl, mhl, 0x01000100u); mv.y = __builtin_amdgcn_perm(mhl, mhl, 0x03020302u); }
      if (s == 13) { if (g) s2 = 0.f; mv.z = split_hl(s2); }
      if (s == 15) { const float rs = __builtin_amdgcn_rcpf(sig); k0 = -2.3022082f * rs; k1 = -0.10294324f * rs * rs * rs; }
      if (s == 14) mv.w = pk2(s2 - bflo(mv.z) - bfhi(mv.z), 0.f);
      c[5] = mfma16(pre[s][0], a[s], c[5]);
      c[6] = mfma16(pre[s][1], a[s], c[6]);
      __builtin_amdgcn_sched_group_barrier(0x8, 1, 0);
      __builtin_amdgcn_sched_group_barrier(0x402, 2, 0);
      __builtin_amdgcn_sched_group_barrier(0x8, 1, 0);
      __builtin_amdgcn_sched_group_barrier(0x402, 2, 0);
#pragma unroll
      for (int nb = 0; nb < 5; ++nb) {
        if (s < 15) bf[(s + 1) & 1][nb] = W1_FRAG(s + 1, nb);
        else bf[0][nb] = *(LAS v4u*)(tabp + nb * 256);
        c[nb] = mfma16(bf[s & 1][nb], a[s], c[nb]);
        __builtin_amdgcn_sched_group_barrier(0x8, 1, 0);
        __builtin_amdgcn_sched_group_barrier(0x100, 2, 0);
        __builtin_amdgcn_sched_group_barrier(0x402, 1, 0);
      }
      if (s == 15) { pre[0][0] = *(LAS v4u*)(tabp + 5 * 256); pre[0][1] = *(LAS v4u*)(tabp + 6 * 256); }
      SCHED();
      if (s == 5) lds_post(cn + 6, l);
      if (s == 14) lds_post(cn, l);
    }
#pragma unroll
    for (int nb = 0; nb < 7; ++nb) c[nb] = mfma16(nb < 5 ? bf[0][nb] : pre[0][nb - 5], mv, c[nb]);
    if (team == 0) {
      lds_wait(cn + 6, 8);
#pragma unroll
      for (int t = 0; t < 12; ++t) dma16(s2w, L0 + (t >> 2) * 16384 + kq * 4096 + (t & 3) * 1024, w2u, cb2 * 128 + (kq * 448 + 2 * 32 + t * 8) * 1024);
    }
    v2u gp[7];
#pragma unroll
    for (int nb = 0; nb < 7; ++nb) {
      float y[4];
#pragma unroll
      for (int e = 0; e < 4; ++e) { const float v = c[nb][e]; y[e] = v * __builtin_amdgcn_rcpf(fmaf(__builtin_amdgcn_exp2f(v * fmaf(v * v, k1, k0)), sig, sig)); }
      gp[nb] = v2u{pk2(y[0], y[1]), pk2(y[2], y[3])};
    }
    const int gb = GF_OFF + ((tg >> 1) * 112 + j2 * 7) * 1024;
    const int gv = (g >> 1) * 1024 + ((g & 1) * 32 + (w & 1) * 16 + tk) * 16, go = ((g >> 1) * 32 + (w & 1) * 16 + tk) * 16 + (g & 1) * 8;
    if (j2 & 1) {
      st64c(gp[0], rws, go, gb);
#pragma unroll
      for (int m = 0; m < 3; ++m) st128c(v4u{gp[1 + 2 * m].x, gp[1 + 2 * m].y, gp[2 + 2 * m].x, gp[2 + 2 * m].y}, rws, gv, gb + (1 + 2 * m) * 1024);
    } else {
#pragma unroll
      for (int m = 0; m < 3; ++m) st128c(v4u{gp[2 * m].x, gp[2 * m].y, gp[2 * m + 1].x, gp[2 * m + 1].y}, rws, gv, gb + 2 * m * 1024);
      st64c(gp[6], rws, go, gb + 6 * 1024);
    }
  }
  {
    LAS char* ww = opq(L + kq * 4096 + team * 2048 + l * 16);
    LAS char* wwh = opq(ww + 7 * 16384);
    const int h = l >> 5, q = tk >> 2;
    constexpr int SL[14] = {7, 8, 0, 1, 2, 3, 4, 5, 6, 7, 8, 0, 1, 2};
    constexpr int WIN = 6;
    v4u a[2][14];
    f32x16 c[2];
    f32x4 r0, r1;
    v4u hh; char* op; LAS char *pa0, *pw0;
    const int gbase = pin(GF_OFF + ((i2 * 4 + team * 2 + th) * 112 + kq * 28) * 1024);
    GAS u64* fp = pin(flag_ptr(ws, 3 + team, i2, l));
#define G2_LOAD(s) _Pragma("unroll") for (int m = 0; m < 2; ++m) a[m][s] = ld128c(rws, l * 16 + (((s) & 1) * 2 + m) * 1024, gbase + ((s) >> 1) * 4096);
    LAS char *wb[3][2], *wo_[2][2];
#pragma unroll
    for (int nb = 0; nb < 2; ++nb) {
      LAS char* wa = L + kq * 4096 + q * 128 + (((nb * 4 + (g & 1) * 2 + ((l >> 1) & 1)) ^ ((q & 2) << 1)) * 16) + (l & 1) * 8;
#pragma unroll
      for (int r3 = 0; r3 < 3; ++r3) wb[r3][nb] = opq(wa + r3 * 49152 + h * 512);
      wo_[0][nb] = opq(wa + SL[3] * 16384 + h * 1024);
      wo_[1][nb] = opq(wa + SL[10] * 16384 + h * 1024);
    }
#define G2_FRAG(s, f) (((s) == 3 || (s) == 10) ? trfrag(wo_[(s) == 10][(f) & 1] + ((f) >> 1) * 2048, 512) \
                                               : trfrag(wb[SL[s] / 3][(f) & 1] + (SL[s] % 3) * 16384 + ((f) >> 1) * 1024, 2048))
    LAS unsigned* mine = cn + 8 + team * 4 + kq;
    LAS unsigned* other = cn + 8 + (team ^ 1) * 4 + kq;
    v4u pf[5][4], bf[2][4];
#define G2_PRE() do { _Pragma("unroll") for (int s = 0; s < 5; ++s) _Pragma("unroll") for (int nb = 0; nb < 4; ++nb) pf[s][nb] = G2_FRAG(s, nb); \
      lds_post(mine, l); } while (0)
#define G2_OVER() do { _Pragma("unroll") for (int t = 0; t < 10; ++t) *(LAS v4u*)((t < 4 ? wwh : ww) + (t < 4 ? (t >> 1) : (t >> 1) - 2) * 16384 + (t & 1) * 1024) = w2r[t]; \
      lds_post(mine, l); } while (0)
    {
      hh = *(LAS v4u*)(L + L_H + orow * 144 + oc * 16);
      r0 = f32x4{bflo(bb2.x) + bflo(hh.x), bfhi(bb2.x) + bfhi(hh.x), bflo(bb2.y) + bflo(hh.y), bfhi(bb2.y) + bfhi(hh.y)};
      r1 = f32x4{bflo(bb2.z) + bflo(hh.z), bfhi(bb2.z) + bfhi(hh.z), bflo(bb2.w) + bflo(hh.w), bfhi(bb2.w) + bfhi(hh.w)};
      pa0 = L + (3 + (orow >> 4)) * 16384 + (orow & 15) * 256 + (((2 * oc) ^ (orow & 15)) * 16);
      pw0 = L + (3 + team * 2 + (g & 1)) * 16384 + kq * 4096 + tk * 256 + ((h ^ tk) * 16);
      op = out + (size_t)(i2 * 128 + (orow >> 5) * 64 + th * 32 + (orow & 31)) * 1024 + cb2 * 128 + oc * 16;
      asm volatile("" : "+v"(op), "+v"(r0), "+v"(r1), "+v"(pa0), "+v"(pw0));
    }
    const Flag fl = flag_prep(3 + team, i2, j2, tag, l);
    WAITV(0);
    const bool last = lds_inc(cn + 1 + team, l) == 3;
    if (last) st128c(fl.d, rws, fl.o, 0);
    if (team == 0) {
      lds_wait(cn, 8);
      lds_post(mine, l);
#pragma unroll
      for (int t = 3; t < 7; ++t) {
#pragma unroll
        for (int u = 0; u < 4; ++u) dma16(s2w, L0 + t * 16384 + kq * 4096 + u * 1024, w2u, cb2 * 128 + (kq * 448 + (2 + t) * 32 + u * 8) * 1024);
      }
    } else lds_wait(other, 1);
    G2_PRE();
    if (team) { lds_wait(other, 2); G2_OVER(); }
    if (last) { flag_wait(fp, tag); lds_post(cn + 3 + team, l); } else lds_wait(cn + 3 + team, 1);
#pragma unroll
    for (int s = 0; s < WIN; ++s) { G2_LOAD(s) SCHED(); }
    c[0] = f32x16{0}; c[1] = f32x16{0};
#pragma unroll
    for (int s = 0; s < 14; ++s) {
      if (s + WIN < 14) { G2_LOAD(s + WIN) }
      if (s == 3 && team) lds_wait(other, 3);
      if (s == 4 && team == 0) { lds_wait(other, 1); G2_OVER(); }
      if (s == 8) { lds_post(mine, l); if (!team) lds_wait(other, 2); }
      if (s == 12 && team) lds_wait(other, 4);
#pragma unroll
      for (int nb = 0; nb < 4; ++nb) {
        if (s >= 4 && s < 13) bf[(s + 1) & 1][nb] = G2_FRAG(s < 13 ? s + 1 : 0, nb);
        const v4u wf = s < 5 ? pf[s < 5 ? s : 0][nb] : bf[s & 1][nb];
        c[nb & 1] = mfma32(wf, a[nb >> 1][s], c[nb & 1]);
        __builtin_amdgcn_sched_group_barrier(0x8, 1, 0);
        __builtin_amdgcn_sched_group_barrier(0x100, 2, 0);
      }
      SCHED();
    }
    if (!team) lds_wait(other, 3);
#pragma unroll
    for (int nb = 0; nb < 2; ++nb)
#pragma unroll
      for (int gg = 0; gg < 4; ++gg)
        *(LAS f32x4*)(size_t)((unsigned)(size_t)pw0 ^ (nb * 128 + gg * 32)) = f32x4{c[nb][4 * gg], c[nb][4 * gg + 1], c[nb][4 * gg + 2], c[nb][4 * gg + 3]};
    LBAR();
#pragma unroll
    for (int s = 0; s < 4; ++s) { r0 += *(LAS f32x4*)(pa0 + s * 4096); r1 += *(LAS f32x4*)((LAS char*)(size_t)((unsigned)(size_t)pa0 ^ 16) + s * 4096); }
    { const v4u ov = {pk2(r0[0], r0[1]), pk2(r0[2], r0[3]), pk2(r1[0], r1[1]), pk2(r1[2], r1[3])};
      asm volatile("global_store_dwordx4 %0, %1, off sc1\n\ts_nop 1" ::"v"(op), "v"(ov) : "memory"); }
    if (blockIdx.x == 0 && w == 0) __hip_atomic_store((GAS u64*)(ws + EP_OFF), tag, __ATOMIC_RELAXED, __HIP_MEMORY_SCOPE_AGENT);
    asm volatile(".Lfused_end:");
  }
}

extern "C" void kernel_launch(void* const* d_in, const int* in_sizes, int n_in, void* d_out, int out_size, void* d_ws, size_t ws_size,
                              hipStream_t stream) {
  (void)hipFuncSetAttribute((const void*)k_fused, hipFuncAttributeMaxDynamicSharedMemorySize, 163840);
  k_fused<<<256, 512, 163840, stream>>>((const char*)d_in[0], (const char*)d_in[1], (const char*)d_in[2], (const char*)d_in[4], (const char*)d_in[5],
                                        (const char*)d_in[6], (const char*)d_in[7], (char*)d_ws, (char*)d_out);
}
```
